```python
import torch

def _softmax(x, axis=-1):
    return torch.softmax(x, dim=axis)
def _gelu(x):
    return torch.nn.functional.gelu(x, approximate="tanh")
def _silu(x):
    return torch.nn.functional.silu(x)
def _sigmoid(x):
    return torch.sigmoid(x)
def _layernorm(x, axis=-1, eps=1e-5):
    m = x.mean(dim=axis, keepdim=True)
    v = x.var(dim=axis, keepdim=True, unbiased=False)
    return (x - m) / torch.sqrt(v + eps)
def _scatter_add(tgt, idx, src, axis=0):
    return tgt.clone().index_add_(axis, idx, src)
def _segment_sum(src, seg, n):
    out = torch.zeros((n,) + src.shape[1:], dtype=src.dtype)
    return out.index_add_(0, seg, src)
def _pad(x, pw):
    flat = tuple(v for pair in reversed(pw) for v in pair)
    return torch.nn.functional.pad(x, flat)

kernel_name = 'recurrent_scan__ssm_conv_s4__mixer_channel_mlp__prompt'

def setup_inputs():

    _w = (lambda a: (lambda t: t.to(torch.bfloat16) if t.is_floating_point() else t)(torch.as_tensor(a)))
    def _A_setup():
        B, L, D, N = 1, 2048, 640, 128
        A_re = -(lambda _x: torch.exp(torch.as_tensor(_x)))((lambda *s: torch.randn(*s, dtype=torch.bfloat16) * 3.2)(D, N))
        A_im = torch.pi * (lambda *s: torch.randn(*s, dtype=torch.bfloat16) * 0.05)(D, N)
        B_re = (lambda *s: torch.randn(*s, dtype=torch.bfloat16) * 0.2)(D, N)
        B_im = (lambda *s: torch.randn(*s, dtype=torch.bfloat16) * 0.2)(D, N)
        C_re = (lambda *s: torch.randn(*s, dtype=torch.bfloat16) * 0.2)(D, N)
        C_im = (lambda *s: torch.randn(*s, dtype=torch.bfloat16) * 0.2)(D, N)
        log_dt = (lambda *s: torch.randn(*s, dtype=torch.bfloat16) * 0.05)(D) * 0.5 - 4.0
        Dskip = (lambda *s: torch.randn(*s, dtype=torch.bfloat16) * 0.05)(D)
        return {
            "u": _w((lambda *s: torch.randn(*s, dtype=torch.bfloat16) * 0.05)(B, L, D)),
            "A_re": _w(A_re),
            "A_im": _w(A_im),
            "B_re": _w(B_re),
            "B_im": _w(B_im),
            "C_re": _w(C_re),
            "C_im": _w(C_im),
            "log_dt": _w(log_dt),
            "Dskip": _w(Dskip),
        }
    _A_in = _A_setup()

    def _B_setup():
        B, S, D, F = 1, 2048, 640, 1728
        return {
            "x": _w((lambda *s: torch.randn(*s, dtype=torch.bfloat16) * 0.05)(B, S, D)),
            "w1": _w((lambda *s: torch.randn(*s, dtype=torch.bfloat16) * 3.2)(D, F) * (D ** -0.5)),
            "b1": _w((lambda *s: torch.randn(*s, dtype=torch.bfloat16) * 0.8)(F)),
            "w2": _w((lambda *s: torch.randn(*s, dtype=torch.bfloat16) * 0.05)(F, D) * (F ** -0.5)),
            "b2": _w((lambda *s: torch.randn(*s, dtype=torch.bfloat16) * 0.05)(D)),
        }
    _B_in = _B_setup()

    return {
        "A_u": _A_in["u"],
        "A_A_re": _A_in["A_re"],
        "A_A_im": _A_in["A_im"],
        "A_B_re": _A_in["B_re"],
        "A_B_im": _A_in["B_im"],
        "A_C_re": _A_in["C_re"],
        "A_C_im": _A_in["C_im"],
        "A_log_dt": _A_in["log_dt"],
        "A_Dskip": _A_in["Dskip"],
        "B_x": _B_in["x"],
        "B_w1": _B_in["w1"],
        "B_b1": _B_in["b1"],
        "B_w2": _B_in["w2"],
        "B_b2": _B_in["b2"],
    }

def reference(A_u, A_A_re, A_A_im, A_B_re, A_B_im, A_C_re, A_C_im, A_log_dt, A_Dskip, B_x, B_w1, B_b1, B_w2, B_b2):
    def _A_ref(u, A_re, A_im, B_re, B_im, C_re, C_im, log_dt, Dskip):
        Bsz, L, D = u.shape
        dt = torch.exp(log_dt)[:, None]
        A = A_re + 1j * A_im
        Bc = B_re + 1j * B_im
        Cc = C_re + 1j * C_im
        logdA = dt * A
        dB = dt * Bc
        l = (lambda x, dt: x.to(dt))(torch.arange(L), A_re.dtype)
        P = torch.exp(logdA[:, :, None] * l[None, None, :])
        K = torch.einsum("dn,dnl,dn->dl", Cc, P, dB).real
        L2 = 2 * L
        Kp = _pad(K, ((0, 0), (0, L)))
        up = (lambda x, *d: x.permute(*d))(u, 0, 2, 1)
        up = _pad(up, ((0, 0), (0, 0), (0, L)))
        Kf = (lambda x, axes=(-2,-1): torch.fft.rfft2(x.float(), dim=axes))(Kp[:, None, :], axes=(-2, -1))
        Uf = (lambda x, axes=(-2,-1): torch.fft.rfft2(x.float(), dim=axes))(up[:, :, None, :], axes=(-2, -1))
        Yf = Uf * Kf[None, :, :, :]
        y = (lambda x, s, axes=(-2,-1): torch.fft.irfft2(x, s=s, dim=axes).to(torch.bfloat16))(Yf, (1, L2), axes=(-2, -1))[:, :, 0, :L]
        y = (lambda x, *d: x.permute(*d))(y, 0, 2, 1) + u * Dskip[None, None, :]
        return y

    def _B_ref(x, w1, b1, w2, b2):
        y = _layernorm(x, axis=-1)
        h = _gelu(torch.einsum("bsd,df->bsf", y, w1) + b1)
        return torch.einsum("bsf,fd->bsd", h, w2) + b2

    x = A_u
    xn = _layernorm(x, axis=-1)
    s = _A_ref(xn, A_A_re, A_A_im, A_B_re, A_B_im, A_C_re, A_C_im, A_log_dt, A_Dskip)
    g = _silu(_B_ref(xn, B_w1, B_b1, B_w2, B_b2))
    return x + s * g
```

```cpp
#include <hip/hip_runtime.h>
#include <hip/hip_bf16.h>
#include <stdint.h>

#define ROUND_L 1
#define EMU_BF16 1

#define L_ 2048
#define D_ 640
#define N_ 128
#define F_ 1728

typedef unsigned short u16;
__device__ __forceinline__ float bf2f(u16 v){ return __uint_as_float(((unsigned)v)<<16); }
__device__ __forceinline__ u16 f2bf(float f){ unsigned u=__float_as_uint(f); u = (u + 0x7FFF + ((u>>16)&1))>>16; return (u16)u; }
__device__ __forceinline__ float rb(float f){
 return bf2f(f2bf(f));
}

__device__ float block_sum(float v, float* sh){
  for(int o=32;o>0;o>>=1) v += __shfl_xor(v,o);
  return v;
}
__global__ void ln_kernel(const u16* x, u16* xn, u16* yn){
  int row = blockIdx.x; int lane = threadIdx.x;
  float v[10];
  float s=0;
  for(int i=0;i<10;i++){ v[i]=bf2f(x[row*D_+i*64+lane]); s+=v[i]; }
  s = block_sum(s,0); float m = s/D_;
  float q=0; for(int i=0;i<10;i++){ float d=v[i]-m; q+=d*d; }
  q = block_sum(q,0); float var=q/D_;
  float mb=rb(m), vb=rb(var);
  float den = rb(sqrtf(rb(vb+1e-5f)));
  s=0;
  for(int i=0;i<10;i++){ v[i]=bf2f(f2bf(rb(v[i]-mb)/den)); xn[row*D_+i*64+lane]=f2bf(v[i]); s+=v[i]; }
  s = block_sum(s,0); m = s/D_;
  q=0; for(int i=0;i<10;i++){ float d=v[i]-m; q+=d*d; }
  q = block_sum(q,0); var=q/D_;
  mb=rb(m); vb=rb(var);
  den = rb(sqrtf(rb(vb+1e-5f)));
  for(int i=0;i<10;i++){ yn[row*D_+i*64+lane]=f2bf(rb(v[i]-mb)/den); }
}
__global__ void kgen_kernel(const u16* Are,const u16* Aim,const u16* Bre,const u16* Bim,const u16* Cre,const u16* Cim,const u16* logdt,float* K){
  int d=blockIdx.x; int l=blockIdx.y*256+threadIdx.x;
  float dt = rb(expf(bf2f(logdt[d])));
  float lf = (float)l;
  lf = bf2f(f2bf(lf));
  float acc=0;
  for(int n=0;n<N_;n++){
    float a=dt*bf2f(Are[d*N_+n]), b=dt*bf2f(Aim[d*N_+n]);
    float br=dt*bf2f(Bre[d*N_+n]), bi=dt*bf2f(Bim[d*N_+n]);
    float cr=bf2f(Cre[d*N_+n]), ci=bf2f(Cim[d*N_+n]);
    float wr=cr*br-ci*bi, wi=cr*bi+ci*br;
    float e=expf(a*lf); float sn,cs; sincosf(b*lf,&sn,&cs);
    acc += e*(wr*cs-wi*sn);
  }
  K[d*L_+l]=acc;
}
__global__ void conv_kernel(const float* K,const u16* xn,const u16* Dskip,u16* s){
  int d=blockIdx.x*64+threadIdx.x; int t=blockIdx.y;
  float acc=0;
  for(int l=0;l<=t;l++) acc += K[d*L_+l]*bf2f(xn[(t-l)*D_+d]);
  float y=rb(acc);
  float sk=rb(bf2f(xn[t*D_+d])*bf2f(Dskip[d]));
  s[t*D_+d]=f2bf(y+sk);
}
__global__ void gemm1_kernel(const u16* yn,const u16* w1,const u16* b1,u16* h){
  int f=blockIdx.x*64+threadIdx.x; int t=blockIdx.y;
  float acc=0;
  for(int k=0;k<D_;k++) acc += bf2f(yn[t*D_+k])*bf2f(w1[k*F_+f]);
  float z=rb(rb(acc)+bf2f(b1[f]));
  float g=0.5f*z*(1.f+tanhf(0.7978845608028654f*(z+0.044715f*z*z*z)));
  h[t*F_+f]=f2bf(g);
}
__global__ void gemm2_kernel(const u16* h,const u16* w2,const u16* b2,const u16* x,const u16* s,u16* out){
  int d=blockIdx.x*64+threadIdx.x; int t=blockIdx.y;
  float acc=0;
  for(int k=0;k<F_;k++) acc += bf2f(h[t*F_+k])*bf2f(w2[k*D_+d]);
  float z=rb(rb(acc)+bf2f(b2[d]));
  float g=rb(z/(1.f+expf(-z)));
  float sg=rb(bf2f(s[t*D_+d])*g);
  out[t*D_+d]=f2bf(bf2f(x[t*D_+d])+sg);
}
extern "C" void kernel_launch(void* const* d_in, const int* in_sizes, int n_in,
                              void* d_out, int out_size, void* d_ws, size_t ws_size,
                              hipStream_t stream) {
  const u16* u=(const u16*)d_in[0];
  char* ws=(char*)d_ws;
  u16* xn=(u16*)ws; u16* yn=(u16*)(ws+4*1024*1024); u16* s=(u16*)(ws+8*1024*1024); u16* h=(u16*)(ws+12*1024*1024); float* K=(float*)(ws+24*1024*1024);
  ln_kernel<<<L_,64,0,stream>>>(u,xn,yn);
  kgen_kernel<<<dim3(D_,L_/256),256,0,stream>>>((const u16*)d_in[1],(const u16*)d_in[2],(const u16*)d_in[3],(const u16*)d_in[4],(const u16*)d_in[5],(const u16*)d_in[6],(const u16*)d_in[7],K);
  conv_kernel<<<dim3(D_/64,L_),64,0,stream>>>(K,xn,(const u16*)d_in[8],s);
  gemm1_kernel<<<dim3(F_/64,L_),64,0,stream>>>(yn,(const u16*)d_in[10],(const u16*)d_in[11],h);
  gemm2_kernel<<<dim3(D_/64,L_),64,0,stream>>>(h,(const u16*)d_in[12],(const u16*)d_in[13],u,s,(u16*)d_out);
}
```

```cpp
#include <hip/hip_runtime.h>
#include <hip/hip_bf16.h>
#include <stdint.h>

namespace fk {

typedef unsigned short u16;
typedef unsigned v4u __attribute__((ext_vector_type(4)));
typedef unsigned v2u __attribute__((ext_vector_type(2)));
typedef float f32x4 __attribute__((ext_vector_type(4)));
typedef float f32x2 __attribute__((ext_vector_type(2)));
typedef __bf16 bf16x8 __attribute__((ext_vector_type(8)));
typedef __bf16 bf16x2 __attribute__((ext_vector_type(2)));
typedef short s16x4 __attribute__((ext_vector_type(4)));
typedef unsigned short u16x2 __attribute__((ext_vector_type(2)));
#define LAS __attribute__((address_space(3)))
#define GAS __attribute__((address_space(1)))
typedef __amdgpu_buffer_rsrc_t rsrc_t;
typedef LAS unsigned char* lptr;

constexpr int L_ = 2048, D_ = 640, N_ = 128, F_ = 1728;
constexpr int KS2 = F_ / 32;

constexpr int WS_EPOCH = 0;
constexpr int WS_F1 = 1024;
constexpr int WS_F2 = 2048;
constexpr int WS_FH = 3072;
constexpr int WS_FS = 4096;
constexpr int WS_STATS = 8192;
constexpr int WS_XNB = WS_STATS + L_ * 8;
constexpr int WS_W2T = WS_XNB + L_ * D_ * 2;
constexpr int WS_HT = WS_W2T + 8 * KS2 * 5 * 1024;
constexpr int WS_ST = WS_HT + 128 * KS2 * 1024;
constexpr int WS_END = WS_ST + L_ * D_ * 2;

constexpr int LDS_RING = 0;
constexpr int LDS_LN = 81920;
constexpr int LDS_PAR_AUX = 69632;
constexpr int KROW = 528;
constexpr int KCH = 53 * KROW;
constexpr int LDS_KS = 98304;
constexpr int KD_STR = 1328;
constexpr int LDS_KD = LDS_KS + 2 * KCH;
constexpr int LDS_PAR_MAIN = LDS_KD + 2 * KD_STR;
constexpr int LDS_CS = LDS_PAR_MAIN + 3072;
constexpr int LDS_B1 = LDS_CS + 1024, LDS_B2 = LDS_B1 + 256, LDS_DT = LDS_B2 + 160, LDS_DK = LDS_DT + 16, LDS_CNT = LDS_DK + 16;
constexpr int LDS_TOTAL = 163840;
static_assert(LDS_CNT + 16 <= LDS_TOTAL, "lds");
constexpr int KC_STR = 4160;
constexpr int KC_SET = 8 * KC_STR;
constexpr int LDS_KCL = LDS_KS + 1024;
constexpr int U_STR = 5120;
constexpr int LDS_U = LDS_KCL + KC_SET;
constexpr int LDS_CP = 2 * KC_SET;
static_assert(LDS_U + 4 * U_STR <= LDS_KD && LDS_CP + 32768 <= LDS_KCL, "lds");
constexpr int LDS_P2 = 0;
constexpr int LDS_P4 = 65536;
constexpr int LDS_S2 = LDS_KCL;
constexpr int S2_STR = 272;

__device__ __forceinline__ float bflo(unsigned v) { return __uint_as_float(v << 16); }
__device__ __forceinline__ float bfhi(unsigned v) { return __uint_as_float(v & 0xffff0000u); }
__device__ __forceinline__ unsigned pk2(float a, float b) {
  f32x2 v = {a, b};
  return __builtin_bit_cast(unsigned, __builtin_convertvector(v, bf16x2));
}
__device__ __forceinline__ float rbf(float a) { return bflo(pk2(a, a)); }
__device__ __forceinline__ f32x4 mfma(v4u a, v4u b, f32x4 c) {
  return __builtin_amdgcn_mfma_f32_16x16x32_bf16(__builtin_bit_cast(bf16x8, a), __builtin_bit_cast(bf16x8, b), c, 0, 0, 0);
}
__device__ __forceinline__ v2u trread(lptr p) {
  return __builtin_bit_cast(v2u, __builtin_amdgcn_ds_read_tr16_b64_v4i16((LAS s16x4*)p));
}
__device__ __forceinline__ v4u mkdesc(const void* p, unsigned bytes) {
  unsigned long long a = (unsigned long long)p;
  return (v4u){(unsigned)a, (unsigned)(a >> 32) & 0xffffu, bytes, 0x00020000u};
}
__device__ __forceinline__ void glds(v4u desc, lptr lds, int voff, int soff) {
  asm volatile("s_mov_b32 m0, %0\n\ts_nop 0\n\tbuffer_load_dwordx4 %1, %2, %3 offen lds"
               :: "s"((unsigned)(unsigned long long)lds), "v"(voff), "s"(desc), "s"(soff) : "memory");
}
#define BAR() asm volatile("s_waitcnt lgkmcnt(0)\n\ts_barrier" ::: "memory")
#define WAITV(n) asm volatile("s_waitcnt vmcnt(%c0)" :: "i"(n) : "memory")
#define FENCE() asm volatile("" ::: "memory")
#define SB() __builtin_amdgcn_sched_barrier(0)
#define SC1 16

#define STAMPV(k)

template <int CTRL, int RM>
__device__ __forceinline__ float dpp_add(float v) {
  return v + __builtin_bit_cast(float, __builtin_amdgcn_update_dpp(0, __builtin_bit_cast(int, v), CTRL, RM, 0xf, true));
}
__device__ __forceinline__ void wave_sum2(float& a, float& b) {
  a = dpp_add<0xB1, 0xf>(a); b = dpp_add<0xB1, 0xf>(b);
  a = dpp_add<0x4E, 0xf>(a); b = dpp_add<0x4E, 0xf>(b);
  a = dpp_add<0x141, 0xf>(a); b = dpp_add<0x141, 0xf>(b);
  a = dpp_add<0x140, 0xf>(a); b = dpp_add<0x140, 0xf>(b);
  a = dpp_add<0x142, 0xa>(a); b = dpp_add<0x142, 0xa>(b);
  a = dpp_add<0x143, 0xc>(a); b = dpp_add<0x143, 0xc>(b);
  a = __builtin_bit_cast(float, __builtin_amdgcn_readlane(__builtin_bit_cast(int, a), 63));
  b = __builtin_bit_cast(float, __builtin_amdgcn_readlane(__builtin_bit_cast(int, b), 63));
}

__device__ __forceinline__ f32x2 cmul(f32x2 a, f32x2 e) {
  f32x2 r = {-e.y, e.x};
  return __builtin_elementwise_fma(a.yy, r, a.xx * e);
}
__device__ __forceinline__ f32x2 cexp_l(float a2, float bt, float lam) {
  float m = __builtin_amdgcn_exp2f(a2 * lam);
  float ph = __builtin_amdgcn_fractf(bt * lam);
  f32x2 r = {m * __builtin_amdgcn_cosf(ph), m * __builtin_amdgcn_sinf(ph)};
  return r;
}

__device__ __forceinline__ bool flags_ok(v4u f, int n, unsigned tag, int lane) {
  int i = lane * 4;
  bool ok = (i >= n || f.x == tag) && (i + 1 >= n || f.y == tag) && (i + 2 >= n || f.z == tag) && (i + 3 >= n || f.w == tag);
  return __all(ok);
}
__device__ __forceinline__ void poll_flags(rsrc_t wsr, int off, int n, unsigned tag, int lane) {
#pragma unroll 1
  for (int spin = 0; spin < (1 << 18); ++spin) {
    v4u f = __builtin_amdgcn_raw_buffer_load_b128(wsr, off + lane * 16, 0, SC1);
    if (flags_ok(f, n, tag, lane)) break;
    __builtin_amdgcn_s_sleep(1);
  }
  FENCE();
}

struct Ctx {
  lptr smem;
  rsrc_t wsr;
  int tid, lane, wave, l15, g;
  int w2off;
  int w2last;
  unsigned tag;
};

template <int I>
__device__ __forceinline__ void ldw2(const Ctx& c, v4u (&W2)[35]) {
  constexpr int s = I / 5, j = I % 5;
  W2[I] = __builtin_amdgcn_raw_buffer_load_b128(c.wsr, WS_W2T + c.lane * 16 + j * 1024, s < 6 ? c.w2off + s * 5120 : c.w2last, SC1);
}
template <int LO, int HI>
__device__ __forceinline__ void ldw2r(const Ctx& c, v4u (&W2)[35]) {
  if constexpr (LO < HI) { ldw2<LO>(c, W2); ldw2r<LO + 1, HI>(c, W2); }
}

template <int LO, int HI>
__device__ __forceinline__ void conv_rho(const Ctx& c, lptr tb, lptr ub, f32x4 (&acc)[4], v4u (&W2)[35]) {
  constexpr int NL = HI - LO;
  v4u U[8], T[8];
#pragma unroll
  for (int p = 0; p < 8; ++p) {
    U[p] = *(LAS v4u*)(ub + p * 512);
    v2u t0 = *(volatile LAS v2u*)(tb + (7 - p) * 512), t1 = *(volatile LAS v2u*)(tb + (7 - p) * 512 + 8);
    T[p] = (v4u){t0.x, t0.y, t1.x, t1.y};
  }
  SB();
#pragma unroll
  for (int p = 0; p <= 1; ++p) acc[0] = mfma(T[p], U[1 - p], acc[0]);
#pragma unroll
  for (int p = 0; p <= 3; ++p) acc[1] = mfma(T[p], U[3 - p], acc[1]);
  SB();
  ldw2r<LO, LO + (NL + 2) / 3>(c, W2);
  SB();
#pragma unroll
  for (int p = 0; p <= 5; ++p) acc[2] = mfma(T[p], U[5 - p], acc[2]);
  SB();
  ldw2r<LO + (NL + 2) / 3, LO + (2 * NL + 2) / 3>(c, W2);
  SB();
#pragma unroll
  for (int p = 0; p < 4; ++p) acc[3] = mfma(T[p], U[7 - p], acc[3]);
  SB();
  ldw2r<LO + (2 * NL + 2) / 3, HI>(c, W2);
  SB();
#pragma unroll
  for (int p = 4; p <= 7; ++p) acc[3] = mfma(T[p], U[7 - p], acc[3]);
  SB();
}

template <int LO, int HI, bool SIG>
__device__ __forceinline__ void conv_round(const Ctx& c, int kc_off, int u_off, int chpair, int dk_off, int fh_off, v4u (&W2)[35]
) {
  const int ch = c.wave >> 2, rh = (c.wave >> 1) & 1, cso = c.wave & 1;
  constexpr int NL = HI - LO, Q1 = LO + (NL + 3) / 4, Q2 = LO + (2 * NL + 3) / 4, Q3 = LO + (3 * NL + 3) / 4;
  lptr tb = c.smem + kc_off + (ch * 4 + (c.l15 & 3)) * KC_STR + (2028 - 256 * 7 - 32 * (rh * 4 + 3) + 8 * c.g - 4 * (c.l15 >> 2)) * 2;
  lptr ub = c.smem + u_off + ch * U_STR + (512 - 256 * cso - 32 * (rh * 4 + 3) + 16 * c.l15 - 16 + 8 * c.g) * 2;
  f32x4 acc[4];
#pragma unroll
  for (int i = 0; i < 4; ++i) acc[i] = (f32x4){0.f, 0.f, 0.f, 0.f};
  lptr pp = c.smem + LDS_CP + (rh * 2 + ch) * 8192 + (1 - cso) * 1024 + c.g * 256 + ((c.l15 + 4 * c.g) & 15) * 16;
  auto signal = [&]() {
    if constexpr (SIG) {
      WAITV(Q2 - LO);
      if (c.lane == 0) {
        unsigned old = __hip_atomic_fetch_add((LAS unsigned*)(c.smem + LDS_CNT), 1u, __ATOMIC_RELAXED, __HIP_MEMORY_SCOPE_WORKGROUP);
        if (old == 7 && fh_off >= 0) __builtin_amdgcn_raw_buffer_store_b32(c.tag, c.wsr, fh_off, 0, SC1);
      }
      FENCE();
    }
  };
  conv_rho<LO, Q1>(c, tb + 192, ub + 192, acc, W2);
  conv_rho<Q1, Q2>(c, tb + 128, ub + 128, acc, W2);
  signal();
  conv_rho<Q2, Q3>(c, tb + 64, ub + 64, acc, W2);
  conv_rho<Q3, HI>(c, tb, ub, acc, W2);
#pragma unroll
  for (int i = 0; i < 4; ++i) *(LAS f32x4*)(pp + i * 2048) = acc[i];
  if constexpr (SIG) STAMPV(34);
  BAR();
  if constexpr (SIG) STAMPV(35);
  {
    const int a = c.lane >> 2, gg = c.lane & 3;
    lptr rp = c.smem + LDS_CP + c.wave * 1024 + gg * 256 + ((a + 4 * gg) & 15) * 16;
    f32x4 y0 = *(LAS f32x4*)rp + *(LAS f32x4*)(rp + 16384);
    f32x4 y1 = *(LAS f32x4*)(rp + 8192) + *(LAS f32x4*)(rp + 16384 + 8192);
    v2u u0 = *(LAS v2u*)(c.smem + u_off + 1024 + c.tid * 8), u1 = *(LAS v2u*)(c.smem + u_off + U_STR + 1024 + c.tid * 8);
    unsigned dkp = *(LAS unsigned*)(c.smem + dk_off);
    float dk0 = bflo(dkp), dk1 = bfhi(dkp);
    float a0[4] = {bflo(u0.x), bfhi(u0.x), bflo(u0.y), bfhi(u0.y)}, a1[4] = {bflo(u1.x), bfhi(u1.x), bflo(u1.y), bfhi(u1.y)};
    v4u o;
#pragma unroll
    for (int i = 0; i < 4; ++i) {
      unsigned yb = pk2(y0[i], y1[i]), sk = pk2(a0[i] * dk0, a1[i] * dk1);
      o[i] = pk2(bflo(yb) + bflo(sk), bfhi(yb) + bfhi(sk));
    }
    __builtin_amdgcn_raw_buffer_store_b128(o, c.wsr, WS_ST + (chpair * 2048 + c.tid * 4) * 4, 0, SC1);
  }
  FENCE();
}
#define SV

constexpr int W2A = 15, W2B = 28;

__global__ __launch_bounds__(512, 2) void fused_kernel(
    const u16* __restrict__ xg, const u16* __restrict__ pAre, const u16* __restrict__ pAim, const u16* __restrict__ pBre,
    const u16* __restrict__ pBim, const u16* __restrict__ pCre, const u16* __restrict__ pCim, const u16* __restrict__ plogdt,
    const u16* __restrict__ pDsk, const u16* __restrict__ w1g, const u16* __restrict__ b1g, const u16* __restrict__ w2g,
    const u16* __restrict__ b2g, u16* __restrict__ outg, unsigned char* __restrict__ ws) {
  extern __shared__ __attribute__((aligned(16))) unsigned char smem_[];
  lptr smem = (lptr)smem_;
  STAMPV(0);
  asm volatile("" :: "s"(xg), "s"(pAre), "s"(pAim), "s"(pBre), "s"(pBim), "s"(pCre), "s"(pCim), "s"(plogdt), "s"(pDsk), "s"(w1g),
               "s"(b1g), "s"(w2g), "s"(b2g), "s"(outg), "s"(ws));
  const int tid = threadIdx.x, lane = tid & 63;
  const int wave = __builtin_amdgcn_readfirstlane(tid >> 6);
  const int bid = blockIdx.x, xcd = bid & 7, slot = bid >> 3;
  const bool aux = slot >= 28;
  const int l15 = lane & 15, g = lane >> 4;

  const rsrc_t wsr = __builtin_amdgcn_make_buffer_rsrc(ws, 0, WS_END, 0x00020000);
  const rsrc_t xr = __builtin_amdgcn_make_buffer_rsrc((void*)xg, 0, L_ * D_ * 2, 0x00020000);
  const v4u xd = mkdesc(xg, L_ * D_ * 2), w1d = mkdesc(w1g, D_ * F_ * 2), w2d = mkdesc(w2g, F_ * D_ * 2);

  const int tb = xcd * 32 + slot;
  const int ch0 = xcd * 80 + (aux ? 56 + (slot - 28) * 6 : slot * 2);
  const int nround = aux ? 3 : 1;
  const int gm = (xcd >> 1) * 4 + (slot & 3), gn = (xcd & 1) * 7 + (slot >> 2);
  const int mb = xcd * 4 + (slot & 3), nb = slot >> 2;

  unsigned epoch;
  asm volatile("global_load_dword %0, %1, %2 sc1" : "=v"(epoch) : "v"(0), "s"(ws) : "memory");
  {
    int vo = (tb * 8 + wave) * 1280 + lane * 16;
    glds(xd, smem + LDS_LN + wave * 1280, vo, 0);
    if (lane < 16) glds(xd, smem + LDS_LN + wave * 1280 + 1024, vo, 1024);
  }
  const int par_str = aux ? 1536 : 512;
  lptr par = smem + (aux ? LDS_PAR_AUX : LDS_PAR_MAIN);
  if (wave < 6) {
    const u16* src = wave == 0 ? pAre : wave == 1 ? pAim : wave == 2 ? pBre : wave == 3 ? pBim : wave == 4 ? pCre : pCim;
    const v4u pr = mkdesc(src, D_ * N_ * 2);
    int vo = ch0 * 256 + lane * 16;
    if (aux) glds(pr, par + wave * 1536 + 512, vo, 512);
    if (lane < 32) glds(pr, par + wave * par_str, vo, 0);
  } else if (wave == 6) {
    if (lane < 16) glds(mkdesc(b1g, F_ * 2), smem + LDS_B1, gn * 256 + lane * 16, 0);
    if (lane == 0) glds(mkdesc(plogdt, D_ * 2), smem + LDS_DT, ch0 * 2, 0);
  } else {
    if (lane < 10) glds(mkdesc(b2g, D_ * 2), smem + LDS_B2, nb * 160 + lane * 16, 0);
    if (lane == 0) glds(mkdesc(pDsk, D_ * 2), smem + LDS_DK, ch0 * 2, 0);
  }
  asm volatile("s_barrier" ::: "memory");
  const int rot = (((slot & 3) * 5 + 1) >> 1) + (((slot >> 2) * 3) >> 1);
  int xvo[2], wvo[2];
#pragma unroll
  for (int j = 0; j < 2; ++j) {
    int ins = wave * 2 + j;
    int r = ins * 8 + (lane >> 3);
    xvo[j] = (gm * 128 + r) * 1280 + (((lane & 7) ^ ((r >> 1) & 7)) << 4);
    int k = ins * 4 + g;
    wvo[j] = k * 3456 + gn * 256 + ((l15 ^ (((k & 3) << 2) | ((k >> 2) & 3))) << 4);
  }
  auto issue1 = [&](int i, int q) {
    int kc = i + rot;
    kc = kc >= 20 ? kc - 20 : kc >= 10 ? kc - 10 : kc;
    lptr st = smem + LDS_RING + (i % 3) * 32768 + wave * 2048;
    if (q < 2) glds(w1d, st + 16384 + q * 1024, wvo[q], kc * (64 * 3456));
    else glds(xd, st + (q - 2) * 1024, xvo[q - 2], kc * 128);
  };
  const int ax = xcd * 4 + (slot - 28);
  auto issue_pt = [&](int i, int q) {
    int lb = wave + 8 * i;
    int blk = ax * 17 + lb;
    blk = blk < 540 ? blk : 539;
    int s = blk / 10, nq = blk - s * 10;
    glds(w2d, smem + lb * 4096 + q * 1024, (lane >> 3) * 1280 + (lane & 7) * 16, (s * 32 + q * 8) * 1280 + nq * 128);
  };
  auto issue_any = [&](int i, int q0, int q1) {
    if (!aux) { for (int q = q0; q < q1; ++q) issue1(i, q); }
    else { for (int q = q0; q < q1; ++q) issue_pt(i, q); }
  };
  issue_any(0, 0, 4);
  if (tid == 0) *(LAS unsigned*)(smem + LDS_CNT) = 0u;
  if (tid < 16) *(LAS unsigned*)(smem + LDS_KD + (tid >> 3) * KD_STR + (tid & 7) * 4) = 0u;
  STAMPV(1);

  asm volatile("s_waitcnt vmcnt(4)\n\ts_barrier" : "+v"(epoch) :: "memory");
  const unsigned tag = __builtin_amdgcn_readfirstlane(epoch) + 1u;
  STAMPV(2);
  issue_any(1, 0, 2);

  float alpha, beta;
  {
    lptr rp = smem + LDS_LN + wave * 1280;
    v2u a = *(LAS v2u*)(rp + lane * 8), b = *(LAS v2u*)(rp + 512 + lane * 8);
    unsigned c = *(LAS unsigned*)(rp + 1024 + lane * 4);
    f32x2 v[5] = {{bflo(a.x), bfhi(a.x)}, {bflo(a.y), bfhi(a.y)}, {bflo(b.x), bfhi(b.x)}, {bflo(b.y), bfhi(b.y)}, {bflo(c), bfhi(c)}};
    const float sh = bflo(__builtin_amdgcn_readfirstlane(a.x));
    f32x2 s = {0.f, 0.f}, q = {0.f, 0.f};
#pragma unroll
    for (int i = 0; i < 5; ++i) { f32x2 d = v[i] - sh; s += d; q = __builtin_elementwise_fma(d, d, q); }
    float s1 = s.x + s.y, q1 = q.x + q.y;
    wave_sum2(s1, q1);
    float md = s1 * (1.f / D_);
    float var = fmaxf(q1 * (1.f / D_) - md * md, 0.f);
    float m1 = rbf(md + sh), den1 = rbf(__builtin_amdgcn_sqrtf(rbf(rbf(var) + 1e-5f)));
    float r1 = __builtin_amdgcn_rcpf(den1);
    unsigned o[5];
    s = (f32x2){0.f, 0.f}; q = (f32x2){0.f, 0.f};
#pragma unroll
    for (int i = 0; i < 5; ++i) {
      unsigned d = pk2(v[i].x - m1, v[i].y - m1);
      o[i] = pk2(bflo(d) * r1, bfhi(d) * r1);
      f32x2 w = {bflo(o[i]), bfhi(o[i])};
      s += w; q = __builtin_elementwise_fma(w, w, q);
    }
    *(LAS v2u*)(rp + lane * 8) = (v2u){o[0], o[1]};
    *(LAS v2u*)(rp + 512 + lane * 8) = (v2u){o[2], o[3]};
    *(LAS unsigned*)(rp + 1024 + lane * 4) = o[4];
    float s2 = s.x + s.y, q2 = q.x + q.y;
    wave_sum2(s2, q2);
    float mm = s2 * (1.f / D_);
    float var2 = fmaxf(q2 * (1.f / D_) - mm * mm, 0.f);
    float m2 = rbf(mm), den2 = rbf(__builtin_amdgcn_sqrtf(rbf(rbf(var2) + 1e-5f)));
    float r2 = __builtin_amdgcn_rcpf(den2);
    alpha = r1 * r2; beta = (m1 * r1 + m2) * r2;
  }
  STAMPV(3);
  issue_any(1, 2, 4);
  STAMPV(4);

#pragma unroll 1
  for (int rd = 0; rd < nround; ++rd) {
    const int c = wave >> 2;
    {
      const int half = (wave >> 1) & 1, part = wave & 1;
      const int n = half * 64 + lane;
      lptr pp = par + (rd * 2 + c) * 256 + n * 2;
      float dt = rbf(__builtin_amdgcn_exp2f(bflo(*(LAS u16*)(smem + LDS_DT + (rd * 2 + c) * 2)) * 1.44269504088896f));
      float a2 = dt * bflo(*(LAS u16*)pp) * 1.44269504088896f;
      float bt = dt * bflo(*(LAS u16*)(pp + par_str)) * 0.15915494309189535f;
      lptr dst = smem + LDS_KS + c * KCH + n * 4;
      if (part == 0) {
        dst += 21 * KROW;
        f32x2 V[8], G[4];
        V[0] = (f32x2){1.f, 0.f};
        V[1] = cexp_l(a2, bt, 8.f);
        G[1] = cexp_l(a2, bt, 64.f);
        V[2] = cmul(V[1], V[1]); V[3] = cmul(V[2], V[1]); V[4] = cmul(V[2], V[2]);
        V[5] = cmul(V[4], V[1]); V[6] = cmul(V[4], V[2]); V[7] = cmul(V[5], V[2]);
        G[2] = cmul(G[1], G[1]); G[3] = cmul(G[2], G[1]);
#pragma unroll
        for (int j = 0; j < 8; ++j) *(volatile LAS unsigned*)(dst + j * KROW) = pk2(V[j].x, V[j].y);
#pragma unroll
        for (int i = 1; i < 4; ++i) {
          f32x2 t[8];
#pragma unroll
          for (int j = 0; j < 8; ++j) t[j] = j == 0 ? G[i] : cmul(V[j], G[i]);
#pragma unroll
          for (int j = 0; j < 8; ++j) *(volatile LAS unsigned*)(dst + (i * 8 + j) * KROW) = pk2(t[j].x, t[j].y);
        }
      } else {
        f32x2 Bv = {dt * bflo(*(LAS u16*)(pp + 2 * par_str)), -dt * bflo(*(LAS u16*)(pp + 3 * par_str))};
        f32x2 Cv = {bflo(*(LAS u16*)(pp + 4 * par_str)), -bflo(*(LAS u16*)(pp + 5 * par_str))};
        f32x2 W[21];
        W[0] = cmul(Cv, Bv);
        f32x2 E1 = cexp_l(a2, -bt, 1.f), H = cexp_l(a2, -bt, 256.f);
        f32x2 E2 = cmul(E1, E1), E4 = cmul(E2, E2), H2 = cmul(H, H), H4 = cmul(H2, H2);
        W[1] = cmul(W[0], E1); W[2] = cmul(W[0], E2); W[3] = cmul(W[1], E2);
        W[4] = cmul(W[0], E4); W[5] = cmul(W[1], E4); W[6] = cmul(W[2], E4); W[7] = cmul(W[3], E4);
        W[8] = cmul(W[0], H); W[9] = cmul(W[2], H); W[10] = cmul(W[4], H); W[11] = cmul(W[6], H);
        W[12] = cmul(W[0], H2); W[13] = cmul(W[4], H2); W[14] = cmul(W[8], H2); W[15] = cmul(W[10], H2);
        W[16] = cmul(W[0], H4); W[17] = cmul(W[8], H4); W[18] = cmul(W[12], H4); W[19] = cmul(W[14], H4);
        W[20] = cmul(W[16], H4);
#pragma unroll
        for (int j = 0; j < 21; ++j) *(volatile LAS unsigned*)(dst + j * KROW) = pk2(W[j].x, W[j].y);
      }
    }
    if (rd == 0) STAMPV(5);
    BAR();
    if (rd == 0) STAMPV(6);
    if (rd == 0) {
      if (lane == 0)
        __builtin_amdgcn_raw_buffer_store_b64((v2u){__float_as_uint(alpha), __float_as_uint(beta)}, wsr, WS_STATS + (tb * 8 + wave) * 8, 0, SC1);
      for (int grp = wave; grp < 10; grp += 8) {
        lptr tp = smem + LDS_LN + (l15 >> 2) * 1280 + (grp * 64 + g * 16 + (lane & 3) * 4) * 2;
        v2u lo = trread(tp), hi = trread(tp + 4 * 1280);
        __builtin_amdgcn_raw_buffer_store_b128((v4u){lo.x, lo.y, hi.x, hi.y}, wsr, WS_XNB + (tb * 640 + grp * 64 + lane) * 16, 0, SC1);
      }
      FENCE();
    }
    if (rd == 0) STAMPV(7);
    {
      const int rt = (wave >> 1) & 1, ct = wave & 1;
      int ur = rt * 16 + l15; ur = ur < 21 ? ur : 20;
      lptr ap = smem + LDS_KS + c * KCH + ur * KROW + g * 16;
      lptr bp = smem + LDS_KS + c * KCH + (21 + ct * 16 + l15) * KROW + g * 16;
      v4u fa[8], fb[8];
#pragma unroll
      for (int ks = 0; ks < 8; ++ks) { fa[ks] = *(LAS v4u*)(ap + ks * 64); fb[ks] = *(LAS v4u*)(bp + ks * 64); }
      SB();
      f32x4 d = {0.f, 0.f, 0.f, 0.f};
#pragma unroll
      for (int ks = 0; ks < 8; ++ks) d = mfma(fa[ks], fb[ks], d);
      LAS u16* kd = (LAS u16*)(smem + LDS_KD + c * KD_STR + 32);
      const int j = ct * 16 + l15;
      unsigned p01 = pk2(d.x, d.y), p23 = pk2(d.z, d.w);
      if (rt == 0) {
        int i0 = g < 2 ? g * 4 + 8 * j : g == 2 ? 256 + 4 * j : 384 + 2 * j;
        int i2 = g < 3 ? i0 + 2 : i0 + 64;
        *(LAS unsigned*)(kd + i0) = p01;
        *(LAS unsigned*)(kd + i2) = p23;
      } else if (g == 0) {
        kd[512 + j] = (u16)p01; kd[544 + j] = (u16)(p01 >> 16); kd[576 + j] = (u16)p23; kd[608 + j] = (u16)(p23 >> 16);
      } else if (g == 1 && j == 0) {
        kd[640] = (u16)p01;
      }
    }
    if (rd == 0) STAMPV(8);
    if (aux && rd == 0) {
      if (wave == 0) { for (int q = 0; q < 4; ++q) issue_pt(2, q); }
      WAITV(0);
#pragma unroll 1
      for (int i = 0; i < 3; ++i) {
        int lb = wave + 8 * i;
        int blk = ax * 17 + lb;
        if (lb < 17 && blk < 540) {
          int s = blk / 10, nq = blk - s * 10;
#pragma unroll
          for (int cc = 0; cc < 4; ++cc) {
            lptr tp = smem + lb * 4096 + (4 * g + (l15 >> 2)) * 128 + cc * 32 + (lane & 3) * 8;
            v2u lo = trread(tp), hi = trread(tp + 16 * 128);
            int n16 = nq * 4 + cc, nbb = n16 / 5, j = n16 - nbb * 5;
            __builtin_amdgcn_raw_buffer_store_b128((v4u){lo.x, lo.y, hi.x, hi.y}, wsr, WS_W2T + lane * 16 + ((nbb * KS2 + s) * 5 + j) * 1024, 0, SC1);
          }
        }
      }
      FENCE();
    }
    if (aux && rd == 1) WAITV(0);
    BAR();
    if (rd == 0) STAMPV(9);
    if (aux) {
      if (tid == 0 && rd < 2) __builtin_amdgcn_raw_buffer_store_b32(tag, wsr, rd == 0 ? WS_F1 + bid * 4 : WS_F2 + ax * 4, 0, SC1);
    } else {
      issue1(2, 0); issue1(2, 1); issue1(2, 2); issue1(2, 3);
    }
    if (rd == 0) STAMPV(10);
    {
      lptr kc = smem + (rd + 1 == nround ? LDS_KCL : rd * KC_SET) + c * 4 * KC_STR;
      const int q = tid & 255;
      const int l0 = 2047 - 8 * q;
      const int sg = l0 < 256 ? 256 - l0 : 0;
      const float lf = (float)(l0 + sg);
      lptr kb = smem + LDS_KD + c * KD_STR + 32 + 2 * (128 - 0x4300 - sg);
      unsigned P[6];
#pragma unroll
      for (int k = 0; k < 6; ++k) {
        unsigned r = pk2(lf - (float)(2 * k), lf - (float)(2 * k + 1));
        u16x2 v;
        v.x = *(LAS u16*)(kb + 2 * (r & 0xffffu));
        v.y = k < 5 ? *(LAS u16*)(kb + 2 * (r >> 16)) : (u16)0;
        P[k] = __builtin_bit_cast(unsigned, v);
      }
      unsigned Q[5];
#pragma unroll
      for (int k = 0; k < 5; ++k) Q[k] = __builtin_amdgcn_alignbit(P[k + 1], P[k], 16);
      *(LAS v4u*)(kc + 0 * KC_STR + q * 16) = (v4u){Q[1], Q[2], Q[3], Q[4]};
      *(LAS v4u*)(kc + 1 * KC_STR + q * 16) = (v4u){P[1], P[2], P[3], P[4]};
      *(LAS v4u*)(kc + 2 * KC_STR + q * 16) = (v4u){Q[0], Q[1], Q[2], Q[3]};
      *(LAS v4u*)(kc + 3 * KC_STR + q * 16) = (v4u){P[0], P[1], P[2], P[3]};
      if (q < 8) *(LAS v4u*)(kc + (q >> 1) * KC_STR + 4096 + (q & 1) * 16) = (v4u){0u, 0u, 0u, 0u};
    }
    if (rd == 0) STAMPV(11);
  }
  if (tid < 256) *(LAS v4u*)(smem + LDS_U + (tid >> 6) * U_STR + (tid & 63) * 16) = (v4u){0u, 0u, 0u, 0u};
  STAMPV(12);

  const int s0 = wave * 7 - (wave > 6 ? 1 : 0);
  Ctx cx;
  cx.smem = smem; cx.wsr = wsr; cx.tid = tid; cx.lane = lane; cx.wave = wave; cx.l15 = l15; cx.g = g; cx.tag = tag;
  cx.w2off = (nb * KS2 + s0) * 5120;
  cx.w2last = (nb * KS2 + (s0 + 6 < KS2 ? s0 + 6 : KS2 - 1)) * 5120;
  v4u W2[35];
  const int ugo = WS_XNB + (((wave & 3) * 64 + lane) * 640 + ch0 + (wave >> 2)) * 16;
  const int uwo = (wave >> 2) * U_STR + 1024 + ((wave & 3) * 64 + lane) * 16;

  if (!aux) {
    const int fg = wave >> 1, th = wave & 1;
    lptr xb[2];
    lptr wb[2][2][2];
#pragma unroll
    for (int ks = 0; ks < 2; ++ks) xb[ks] = smem + LDS_RING + (th * 64 + l15) * 128 + (((4 * (ks ^ th) + g) ^ (l15 >> 1)) << 4);
#pragma unroll
    for (int ft = 0; ft < 2; ++ft)
#pragma unroll
      for (int tp = 0; tp < 2; ++tp) {
        int q = l15 >> 2, p = lane & 3;
        int row = 8 * g + 4 * tp + q;
        int chn = 4 * fg + 2 * ft + (p >> 1);
#pragma unroll
        for (int ks = 0; ks < 2; ++ks)
          wb[ks][ft][tp] = smem + LDS_RING + 16384 + (ks ^ th) * 8192 + 256 * row + ((chn ^ ((q << 2) | ((2 * g + tp) & 3))) << 4) + 8 * (p & 1);
      }
    f32x4 acc1[2][4], cs[2];
#pragma unroll
    for (int a = 0; a < 2; ++a) {
      cs[a] = (f32x4){0.f, 0.f, 0.f, 0.f};
#pragma unroll
      for (int b = 0; b < 4; ++b) acc1[a][b] = (f32x4){0.f, 0.f, 0.f, 0.f};
    }
    const v4u ones = {0x3f803f80u, 0x3f803f80u, 0x3f803f80u, 0x3f803f80u};
    v4u XA[2][2][4], WA[2][2][2];
    auto rdw = [&](int i, int ks) {
      const int so = (i % 3) * 32768, bf = i & 1;
#pragma unroll
      for (int ft = 0; ft < 2; ++ft) {
        v2u lo = trread(wb[ks][ft][0] + so), hi = trread(wb[ks][ft][1] + so);
        WA[bf][ks][ft] = (v4u){lo.x, lo.y, hi.x, hi.y};
      }
    };
    auto rdx = [&](int i, int ks) {
      const int so = (i % 3) * 32768, bf = i & 1;
#pragma unroll
      for (int tt = 0; tt < 4; ++tt) XA[bf][ks][tt] = *(LAS v4u*)(xb[ks] + so + tt * 2048);
    };
    WAITV(4);
    BAR();
    if (tid == 0) __builtin_amdgcn_raw_buffer_store_b32(tag, wsr, WS_F1 + bid * 4, 0, SC1);
    STAMPV(13);
    rdw(0, 0); rdx(0, 0); rdw(0, 1); rdx(0, 1);
    v4u fl;
    v2u st[4];
    v4u ug;
#pragma unroll
    for (int i = 0; i < 10; ++i) {
      const int bf = i & 1;
      if (i + 1 < 10) {
        if (i == 0) { }
        else if (i == 1) { if (wave == 0) WAITV(5); else WAITV(4); }
        else if (i + 2 < 10) WAITV(4);
        else {
          asm volatile("s_waitcnt vmcnt(0)" : "+v"(fl) :: "memory");
          if (wave < 2) { if (!flags_ok(fl, wave == 0 ? 256 : 32, tag, lane)) poll_flags(wsr, WS_F1 + wave * 1024, wave == 0 ? 256 : 32, tag, lane); }
        }
        BAR();
      }
      SB();
#pragma unroll
      for (int q = 0; q < 4; ++q) {
        const int ks = q >> 1, ft = q & 1;
        if (i + 1 < 10) { if (ft == 0) rdw(i + 1, ks); else rdx(i + 1, ks); }
        SB();
#pragma unroll
        for (int tt = 0; tt < 4; ++tt) acc1[ft][tt] = mfma(WA[bf][ks][ft], XA[bf][ks][tt], acc1[ft][tt]);
        if (ks == 0) cs[ft] = mfma(WA[bf][0][ft], ones, cs[ft]);
        SB();
        if (i + 3 < 10) issue1(i + 3, q);
        if (i == 6 && q == 3) asm volatile("buffer_load_dwordx4 %0, %1, %2, 0 offen sc1" : "=v"(fl) : "v"(WS_F1 + (wave & 1) * 1024 + lane * 16), "s"(mkdesc(ws, WS_END)) : "memory");
        if (i == 8) {
          st[q] = __builtin_amdgcn_raw_buffer_load_b64(wsr, WS_STATS + (gm * 128 + th * 64 + q * 16 + l15) * 8, 0, SC1);
          if (q == 3) ug = __builtin_amdgcn_raw_buffer_load_b128(wsr, ugo, 0, SC1);
        }
        if (i == 9) {
          if (q == 0) ldw2r<0, 1>(cx, W2);
          if (q == 1) ldw2r<1, 2>(cx, W2);
          if (q == 2) ldw2r<2, 3>(cx, W2);
        }
        SB();
      }
      STAMPV(14 + i);
    }
    if (l15 == 0) {
#pragma unroll
      for (int ft = 0; ft < 2; ++ft) *(LAS f32x4*)(smem + LDS_CS + ((wave * 2 + ft) * 4 + g) * 16) = cs[ft];
    }
    BAR();
    STAMPV(26);
    const int sidx = gn * 4 + fg;
    const int hoff = sidx < KS2 ? WS_HT + lane * 16 + ((gm * 8 + th * 4) * KS2 + sidx) * 1024 : WS_END;
    f32x4 csum[2], bb[2];
#pragma unroll
    for (int ft = 0; ft < 2; ++ft) {
      v2u b1v = *(LAS v2u*)(smem + LDS_B1 + (fg * 32 + ft * 16 + g * 4) * 2);
      bb[ft] = (f32x4){bflo(b1v.x), bfhi(b1v.x), bflo(b1v.y), bfhi(b1v.y)};
      csum[ft] = *(LAS f32x4*)(smem + LDS_CS + (((fg * 2) * 2 + ft) * 4 + g) * 16) + *(LAS f32x4*)(smem + LDS_CS + (((fg * 2 + 1) * 2 + ft) * 4 + g) * 16);
    }
#pragma unroll
    for (int tt = 0; tt < 4; ++tt) {
      float al = __uint_as_float(st[tt].x), be = __uint_as_float(st[tt].y);
      v4u hf;
#pragma unroll
      for (int ft = 0; ft < 2; ++ft) {
        f32x4 z = acc1[ft][tt] * al + (bb[ft] - csum[ft] * be);
        f32x4 ar = z * (z * z * (-0.044715f * 2.302208198f) + (-2.302208198f));
        f32x4 o;
#pragma unroll
        for (int e = 0; e < 4; ++e) o[e] = z[e] * __builtin_amdgcn_rcpf(1.f + __builtin_amdgcn_exp2f(ar[e]));
        hf[2 * ft] = pk2(o.x, o.y);
        hf[2 * ft + 1] = pk2(o.z, o.w);
        SB();
        if (tt == 0 && ft == 0) ldw2r<3, 5>(cx, W2);
        if (tt == 0 && ft == 1) ldw2r<5, 6>(cx, W2);
        if (tt == 1 && ft == 0) ldw2r<6, 8>(cx, W2);
        if (tt == 1 && ft == 1) ldw2r<8, 9>(cx, W2);
        if (tt == 2 && ft == 0) ldw2r<9, 11>(cx, W2);
        if (tt == 2 && ft == 1) ldw2r<11, 12>(cx, W2);
        if (tt == 3 && ft == 0) ldw2r<12, 14>(cx, W2);
        if (tt == 3 && ft == 1) ldw2r<14, W2A>(cx, W2);
        SB();
      }
      __builtin_amdgcn_raw_buffer_store_b128(hf, wsr, hoff + tt * (KS2 * 1024), 0, SC1);
      SB();
    }
    *(LAS v4u*)(smem + LDS_U + uwo) = ug;
    STAMPV(28);
  } else {
    if (wave == 0) poll_flags(wsr, WS_F1, 256, tag, lane);
    BAR();
    STAMPV(13);
    v4u ug0 = __builtin_amdgcn_raw_buffer_load_b128(wsr, ugo, 0, SC1);
    v4u ug1 = __builtin_amdgcn_raw_buffer_load_b128(wsr, ugo + 32, 0, SC1);
    v4u ug2 = __builtin_amdgcn_raw_buffer_load_b128(wsr, ugo + 64, 0, SC1);
    *(LAS v4u*)(smem + LDS_U + uwo) = ug0;
    *(LAS v4u*)(smem + LDS_U + 2 * U_STR + uwo) = ug1;
    BAR();
    STAMPV(14);
    conv_round<0, 0, false>(cx, 0, LDS_U, (ch0 >> 1), LDS_DK, -1, W2 SV);
    if (wave == 0) poll_flags(wsr, WS_F2, 32, tag, lane);
    BAR();
    STAMPV(15);
    *(LAS v4u*)(smem + LDS_U + uwo) = ug2;
    conv_round<0, W2A, false>(cx, KC_SET, LDS_U + 2 * U_STR, (ch0 >> 1) + 1, LDS_DK + 4, -1, W2 SV);
    STAMPV(28);
  }
  if (bid == 0 && tid == 64) __builtin_amdgcn_raw_buffer_store_b32(tag, wsr, WS_EPOCH, 0, SC1);
  BAR();
  STAMPV(33);

  conv_round<W2A, W2B, true>(cx, LDS_KCL, LDS_U, (ch0 >> 1) + nround - 1, LDS_DK + (nround - 1) * 4, aux ? -1 : WS_FH + (gm * 16 + gn) * 4, W2 SV);
  STAMPV(36);

  {
    v4u fh = __builtin_amdgcn_raw_buffer_load_b128(wsr, WS_FH + (mb >> 1) * 64 + (lane & 3) * 16, 0, SC1);
    FENCE();
    ldw2r<W2B, 35>(cx, W2);
    FENCE();
    if (!flags_ok(fh, 14, tag, lane & 3)) poll_flags(wsr, WS_FH + (mb >> 1) * 64, 14, tag, lane);
    FENCE();
    STAMPV(45);
    v4u H[2][7];
    const int hb = (mb * 4 * KS2 + s0) * 1024, hl = s0 + 6 < KS2 ? 6144 : 5120;
    auto ldh = [&](int r, int s) {
      H[r & 1][s] = __builtin_amdgcn_raw_buffer_load_b128(wsr, WS_HT + lane * 16, hb + r * (KS2 * 1024) + (s < 6 ? s * 1024 : hl), SC1);
    };
#pragma unroll
    for (int s = 0; s < 7; ++s) ldh(0, s);
#pragma unroll
    for (int s = 0; s < 7; ++s) ldh(1, s);
    FENCE();
    STAMPV(46);
    const int ft_ = lane >> 2, fg_ = lane & 3;
    const int jj = wave < 4 ? wave : 4;
    const int rdo = fg_ * 256 + ((ft_ + 4 * fg_) & 15) * 16;
    const int wro = g * 256 + ((l15 + 4 * g) & 15) * 16;
    v2u b2v = *(LAS v2u*)(smem + LDS_B2 + (jj * 16 + fg_ * 4) * 2);
    auto gate = [&](lptr pb, int stride) -> v2u {
      f32x4 z = (*(LAS f32x4*)(pb) + *(LAS f32x4*)(pb + stride)) + (*(LAS f32x4*)(pb + 2 * stride) + *(LAS f32x4*)(pb + 3 * stride));
      z += (*(LAS f32x4*)(pb + 4 * stride) + *(LAS f32x4*)(pb + 5 * stride)) + (*(LAS f32x4*)(pb + 6 * stride) + *(LAS f32x4*)(pb + 7 * stride));
      unsigned z01 = pk2(z.x, z.y), z23 = pk2(z.z, z.w);
      z01 = pk2(bflo(z01) + bflo(b2v.x), bfhi(z01) + bfhi(b2v.x));
      z23 = pk2(bflo(z23) + bflo(b2v.y), bfhi(z23) + bfhi(b2v.y));
      float zz[4] = {bflo(z01), bfhi(z01), bflo(z23), bfhi(z23)};
#pragma unroll
      for (int e = 0; e < 4; ++e) zz[e] = zz[e] * __builtin_amdgcn_rcpf(1.f + __builtin_amdgcn_exp2f(zz[e] * -1.44269504088896f));
      return (v2u){pk2(zz[0], zz[1]), pk2(zz[2], zz[3])};
    };
    v2u gv[3], xv[4];
    v4u sreg[2], fs;
    const int rl = wave < 4 ? 3 : wave - 4;
    const int xo = ((mb * 64 + ft_) * 640 + nb * 80 + jj * 16 + fg_ * 4) * 2;
#pragma unroll
    for (int r = 0; r < 4; ++r) {
      if (r == 2) fs = __builtin_amdgcn_raw_buffer_load_b128(wsr, WS_FS + lane * 16, 0, SC1);
      if (r == 3) {
#pragma unroll
        for (int i = 0; i < 2; ++i) {
          int it = tid + 512 * i; it = it < 640 ? it : 639;
          sreg[i] = __builtin_amdgcn_raw_buffer_load_b128(wsr, WS_ST + ((nb * 40 + (it >> 4)) * 2048 + mb * 64) * 4 + (it & 15) * 16, 0, SC1);
        }
        xv[3] = __builtin_amdgcn_raw_buffer_load_b64(xr, xo + rl * (16 * 1280), 0, 0);
#pragma unroll
        for (int i = 0; i < 3; ++i) xv[i] = __builtin_amdgcn_raw_buffer_load_b64(xr, xo + i * (16 * 1280), 0, 0);
      }
      SB();
      if (r > 0 && wave < 4) gv[r - 1] = gate(smem + LDS_P2 + ((r - 1) & 1) * 32768 + wave * 1024 + rdo, 4096);
      SB();
      f32x4 acc[5];
#pragma unroll
      for (int j = 0; j < 5; ++j) acc[j] = (f32x4){0.f, 0.f, 0.f, 0.f};
#pragma unroll
      for (int s = 0; s < 6; ++s) {
#pragma unroll
        for (int j = 0; j < 5; ++j) acc[j] = mfma(W2[s * 5 + j], H[r & 1][s], acc[j]);
        SB();
        if (r + 2 < 4 && s > 0) ldh(r + 2, s - 1);
        SB();
      }
      if (wave < 6) {
#pragma unroll
        for (int j = 0; j < 5; ++j) acc[j] = mfma(W2[30 + j], H[r & 1][6], acc[j]);
      }
      SB();
      if (r + 2 < 4) { ldh(r + 2, 5); ldh(r + 2, 6); }
      STAMPV(47 + r * 3);
      if (r == 0) BAR();
#pragma unroll
      for (int j = 0; j < 4; ++j) *(LAS f32x4*)(smem + LDS_P2 + (r & 1) * 32768 + (wave * 4 + j) * 1024 + wro) = acc[j];
      *(LAS f32x4*)(smem + LDS_P4 + r * 8192 + wave * 1024 + wro) = acc[4];
      if (r == 0) WAITV(14);
      if (r == 2 && wave == 7) { if (!flags_ok(fs, 256, tag, lane)) poll_flags(wsr, WS_FS, 256, tag, lane); }
      if (r == 3) {
        *(LAS v4u*)(smem + LDS_S2 + (tid >> 4) * S2_STR + (tid & 15) * 16) = sreg[0];
        if (tid < 128) *(LAS v4u*)(smem + LDS_S2 + (32 + (tid >> 4)) * S2_STR + (tid & 15) * 16) = sreg[1];
      }
      STAMPV(48 + r * 3);
      BAR();
      STAMPV(49 + r * 3);
      if (r == 0 && tid == 0) __builtin_amdgcn_raw_buffer_store_b32(tag, wsr, WS_FS + bid * 4, 0, SC1);
    }
    auto emit = [&](int r, v2u gg, v2u xx) {
      lptr sp = smem + LDS_S2 + (jj * 8 + fg_ * 2) * S2_STR + (r * 16 + ft_) * 4;
      unsigned s01 = *(LAS unsigned*)sp, s23 = *(LAS unsigned*)(sp + S2_STR);
      unsigned p01 = pk2(bflo(s01) * bflo(gg.x), bfhi(s01) * bfhi(gg.x)), p23 = pk2(bflo(s23) * bflo(gg.y), bfhi(s23) * bfhi(gg.y));
      v2u o = {pk2(bflo(xx.x) + bflo(p01), bfhi(xx.x) + bfhi(p01)), pk2(bflo(xx.y) + bflo(p23), bfhi(xx.y) + bfhi(p23))};
      *(GAS v2u*)((GAS unsigned char*)outg + xo + r * (16 * 1280)) = o;
    };
    {
      v2u gl = gate(wave < 4 ? smem + LDS_P2 + 32768 + wave * 1024 + rdo : smem + LDS_P4 + rl * 8192 + rdo, wave < 4 ? 4096 : 1024);
      emit(rl, gl, xv[3]);
    }
    if (wave < 4) {
#pragma unroll
      for (int r = 0; r < 3; ++r) emit(r, gv[r], xv[r]);
    }
  }
}

}

extern "C" void kernel_launch(void* const* d_in, const int* in_sizes, int n_in, void* d_out, int out_size, void* d_ws,
                              size_t ws_size, hipStream_t stream) {
  using namespace fk;
  (void)hipFuncSetAttribute((const void*)fused_kernel, hipFuncAttributeMaxDynamicSharedMemorySize, LDS_TOTAL);
  fused_kernel<<<256, 512, LDS_TOTAL, stream>>>(
      (const u16*)d_in[0], (const u16*)d_in[1], (const u16*)d_in[2], (const u16*)d_in[3], (const u16*)d_in[4], (const u16*)d_in[5],
      (const u16*)d_in[6], (const u16*)d_in[7], (const u16*)d_in[8], (const u16*)d_in[10], (const u16*)d_in[11], (const u16*)d_in[12],
      (const u16*)d_in[13], (u16*)d_out, (unsigned char*)d_ws);
}
```

```cpp
#define PROBE 2
#include <hip/hip_runtime.h>
#include <hip/hip_bf16.h>
#include <stdint.h>

namespace fk {

typedef unsigned short u16;
typedef unsigned v4u __attribute__((ext_vector_type(4)));
typedef unsigned v2u __attribute__((ext_vector_type(2)));
typedef float f32x4 __attribute__((ext_vector_type(4)));
typedef float f32x2 __attribute__((ext_vector_type(2)));
typedef __bf16 bf16x8 __attribute__((ext_vector_type(8)));
typedef __bf16 bf16x2 __attribute__((ext_vector_type(2)));
typedef short s16x4 __attribute__((ext_vector_type(4)));
typedef unsigned short u16x2 __attribute__((ext_vector_type(2)));
#define LAS __attribute__((address_space(3)))
#define GAS __attribute__((address_space(1)))
typedef __amdgpu_buffer_rsrc_t rsrc_t;
typedef LAS unsigned char* lptr;

constexpr int L_ = 2048, D_ = 640, N_ = 128, F_ = 1728;
constexpr int KS2 = F_ / 32;

constexpr int WS_EPOCH = 0;
constexpr int WS_F1 = 1024;
constexpr int WS_F2 = 2048;
constexpr int WS_FH = 3072;
constexpr int WS_FS = 4096;
constexpr int WS_STATS = 8192;
constexpr int WS_XNB = WS_STATS + L_ * 8;
constexpr int WS_W2T = WS_XNB + L_ * D_ * 2;
constexpr int WS_HT = WS_W2T + 8 * KS2 * 5 * 1024;
constexpr int WS_ST = WS_HT + 128 * KS2 * 1024;
constexpr int WS_END = WS_ST + L_ * D_ * 2;

constexpr int LDS_RING = 0;
constexpr int LDS_LN = 81920;
constexpr int LDS_PAR_AUX = 69632;
constexpr int KROW = 528;
constexpr int KCH = 53 * KROW;
constexpr int LDS_KS = 98304;
constexpr int KD_STR = 1328;
constexpr int LDS_KD = LDS_KS + 2 * KCH;
constexpr int LDS_PAR_MAIN = LDS_KD + 2 * KD_STR;
constexpr int LDS_CS = LDS_PAR_MAIN + 3072;
constexpr int LDS_B1 = LDS_CS + 1024, LDS_B2 = LDS_B1 + 256, LDS_DT = LDS_B2 + 160, LDS_DK = LDS_DT + 16, LDS_CNT = LDS_DK + 16;
constexpr int LDS_TOTAL = 163840;
static_assert(LDS_CNT + 16 <= LDS_TOTAL, "lds");
constexpr int KC_STR = 4160;
constexpr int KC_SET = 8 * KC_STR;
constexpr int LDS_KCL = LDS_KS + 1024;
constexpr int U_STR = 5120;
constexpr int LDS_U = LDS_KCL + KC_SET;
constexpr int LDS_CP = 2 * KC_SET;
static_assert(LDS_U + 4 * U_STR <= LDS_KD && LDS_CP + 32768 <= LDS_KCL, "lds");
constexpr int LDS_P2 = 0;
constexpr int LDS_P4 = 65536;
constexpr int LDS_S2 = LDS_KCL;
constexpr int S2_STR = 272;

__device__ __forceinline__ float bflo(unsigned v) { return __uint_as_float(v << 16); }
__device__ __forceinline__ float bfhi(unsigned v) { return __uint_as_float(v & 0xffff0000u); }
__device__ __forceinline__ unsigned pk2(float a, float b) {
  f32x2 v = {a, b};
  return __builtin_bit_cast(unsigned, __builtin_convertvector(v, bf16x2));
}
__device__ __forceinline__ float rbf(float a) { return bflo(pk2(a, a)); }
__device__ __forceinline__ f32x4 mfma(v4u a, v4u b, f32x4 c) {
  return __builtin_amdgcn_mfma_f32_16x16x32_bf16(__builtin_bit_cast(bf16x8, a), __builtin_bit_cast(bf16x8, b), c, 0, 0, 0);
}
__device__ __forceinline__ v2u trread(lptr p) {
  return __builtin_bit_cast(v2u, __builtin_amdgcn_ds_read_tr16_b64_v4i16((LAS s16x4*)p));
}
__device__ __forceinline__ v4u mkdesc(const void* p, unsigned bytes) {
  unsigned long long a = (unsigned long long)p;
  return (v4u){(unsigned)a, (unsigned)(a >> 32) & 0xffffu, bytes, 0x00020000u};
}
__device__ __forceinline__ void glds(v4u desc, lptr lds, int voff, int soff) {
  asm volatile("s_mov_b32 m0, %0\n\ts_nop 0\n\tbuffer_load_dwordx4 %1, %2, %3 offen lds"
               :: "s"((unsigned)(unsigned long long)lds), "v"(voff), "s"(desc), "s"(soff) : "memory");
}
#define BAR() asm volatile("s_waitcnt lgkmcnt(0)\n\ts_barrier" ::: "memory")
#define WAITV(n) asm volatile("s_waitcnt vmcnt(%c0)" :: "i"(n) : "memory")
#define FENCE() asm volatile("" ::: "memory")
#define SB() __builtin_amdgcn_sched_barrier(0)
#define SC1 16

#define STAMPV(k) do { if ((k) == PROBE) probe_t = __builtin_amdgcn_s_memrealtime(); } while (0)

template <int CTRL, int RM>
__device__ __forceinline__ float dpp_add(float v) {
  return v + __builtin_bit_cast(float, __builtin_amdgcn_update_dpp(0, __builtin_bit_cast(int, v), CTRL, RM, 0xf, true));
}
__device__ __forceinline__ void wave_sum2(float& a, float& b) {
  a = dpp_add<0xB1, 0xf>(a); b = dpp_add<0xB1, 0xf>(b);
  a = dpp_add<0x4E, 0xf>(a); b = dpp_add<0x4E, 0xf>(b);
  a = dpp_add<0x141, 0xf>(a); b = dpp_add<0x141, 0xf>(b);
  a = dpp_add<0x140, 0xf>(a); b = dpp_add<0x140, 0xf>(b);
  a = dpp_add<0x142, 0xa>(a); b = dpp_add<0x142, 0xa>(b);
  a = dpp_add<0x143, 0xc>(a); b = dpp_add<0x143, 0xc>(b);
  a = __builtin_bit_cast(float, __builtin_amdgcn_readlane(__builtin_bit_cast(int, a), 63));
  b = __builtin_bit_cast(float, __builtin_amdgcn_readlane(__builtin_bit_cast(int, b), 63));
}

__device__ __forceinline__ f32x2 cmul(f32x2 a, f32x2 e) {
  f32x2 r = {-e.y, e.x};
  return __builtin_elementwise_fma(a.yy, r, a.xx * e);
}
__device__ __forceinline__ f32x2 cexp_l(float a2, float bt, float lam) {
  float m = __builtin_amdgcn_exp2f(a2 * lam);
  float ph = __builtin_amdgcn_fractf(bt * lam);
  f32x2 r = {m * __builtin_amdgcn_cosf(ph), m * __builtin_amdgcn_sinf(ph)};
  return r;
}

__device__ __forceinline__ bool flags_ok(v4u f, int n, unsigned tag, int lane) {
  int i = lane * 4;
  bool ok = (i >= n || f.x == tag) && (i + 1 >= n || f.y == tag) && (i + 2 >= n || f.z == tag) && (i + 3 >= n || f.w == tag);
  return __all(ok);
}
__device__ __forceinline__ void poll_flags(rsrc_t wsr, int off, int n, unsigned tag, int lane) {
#pragma unroll 1
  for (int spin = 0; spin < (1 << 18); ++spin) {
    v4u f = __builtin_amdgcn_raw_buffer_load_b128(wsr, off + lane * 16, 0, SC1);
    if (flags_ok(f, n, tag, lane)) break;
    __builtin_amdgcn_s_sleep(1);
  }
  FENCE();
}

struct Ctx {
  lptr smem;
  rsrc_t wsr;
  int tid, lane, wave, l15, g;
  int w2off;
  int w2last;
  unsigned tag;
};

template <int I>
__device__ __forceinline__ void ldw2(const Ctx& c, v4u (&W2)[35]) {
  constexpr int s = I / 5, j = I % 5;
  W2[I] = __builtin_amdgcn_raw_buffer_load_b128(c.wsr, WS_W2T + c.lane * 16 + j * 1024, s < 6 ? c.w2off + s * 5120 : c.w2last, SC1);
}
template <int LO, int HI>
__device__ __forceinline__ void ldw2r(const Ctx& c, v4u (&W2)[35]) {
  if constexpr (LO < HI) { ldw2<LO>(c, W2); ldw2r<LO + 1, HI>(c, W2); }
}

template <int LO, int HI>
__device__ __forceinline__ void conv_rho(const Ctx& c, lptr tb, lptr ub, f32x4 (&acc)[4], v4u (&W2)[35]) {
  constexpr int NL = HI - LO;
  v4u U[8], T[8];
#pragma unroll
  for (int p = 0; p < 8; ++p) {
    U[p] = *(LAS v4u*)(ub + p * 512);
    v2u t0 = *(volatile LAS v2u*)(tb + (7 - p) * 512), t1 = *(volatile LAS v2u*)(tb + (7 - p) * 512 + 8);
    T[p] = (v4u){t0.x, t0.y, t1.x, t1.y};
  }
  SB();
#pragma unroll
  for (int p = 0; p <= 1; ++p) acc[0] = mfma(T[p], U[1 - p], acc[0]);
#pragma unroll
  for (int p = 0; p <= 3; ++p) acc[1] = mfma(T[p], U[3 - p], acc[1]);
  SB();
  ldw2r<LO, LO + (NL + 2) / 3>(c, W2);
  SB();
#pragma unroll
  for (int p = 0; p <= 5; ++p) acc[2] = mfma(T[p], U[5 - p], acc[2]);
  SB();
  ldw2r<LO + (NL + 2) / 3, LO + (2 * NL + 2) / 3>(c, W2);
  SB();
#pragma unroll
  for (int p = 0; p < 4; ++p) acc[3] = mfma(T[p], U[7 - p], acc[3]);
  SB();
  ldw2r<LO + (2 * NL + 2) / 3, HI>(c, W2);
  SB();
#pragma unroll
  for (int p = 4; p <= 7; ++p) acc[3] = mfma(T[p], U[7 - p], acc[3]);
  SB();
}

template <int LO, int HI, bool SIG>
__device__ __forceinline__ void conv_round(const Ctx& c, int kc_off, int u_off, int chpair, int dk_off, int fh_off, v4u (&W2)[35]
) {
  const int ch = c.wave >> 2, rh = (c.wave >> 1) & 1, cso = c.wave & 1;
  constexpr int NL = HI - LO, Q1 = LO + (NL + 3) / 4, Q2 = LO + (2 * NL + 3) / 4, Q3 = LO + (3 * NL + 3) / 4;
  lptr tb = c.smem + kc_off + (ch * 4 + (c.l15 & 3)) * KC_STR + (2028 - 256 * 7 - 32 * (rh * 4 + 3) + 8 * c.g - 4 * (c.l15 >> 2)) * 2;
  lptr ub = c.smem + u_off + ch * U_STR + (512 - 256 * cso - 32 * (rh * 4 + 3) + 16 * c.l15 - 16 + 8 * c.g) * 2;
  f32x4 acc[4];
#pragma unroll
  for (int i = 0; i < 4; ++i) acc[i] = (f32x4){0.f, 0.f, 0.f, 0.f};
  lptr pp = c.smem + LDS_CP + (rh * 2 + ch) * 8192 + (1 - cso) * 1024 + c.g * 256 + ((c.l15 + 4 * c.g) & 15) * 16;
  auto signal = [&]() {
    if constexpr (SIG) {
      WAITV(Q2 - LO);
      if (c.lane == 0) {
        unsigned old = __hip_atomic_fetch_add((LAS unsigned*)(c.smem + LDS_CNT), 1u, __ATOMIC_RELAXED, __HIP_MEMORY_SCOPE_WORKGROUP);
        if (old == 7 && fh_off >= 0) __builtin_amdgcn_raw_buffer_store_b32(c.tag, c.wsr, fh_off, 0, SC1);
      }
      FENCE();
    }
  };
  conv_rho<LO, Q1>(c, tb + 192, ub + 192, acc, W2);
  conv_rho<Q1, Q2>(c, tb + 128, ub + 128, acc, W2);
  signal();
  conv_rho<Q2, Q3>(c, tb + 64, ub + 64, acc, W2);
  conv_rho<Q3, HI>(c, tb, ub, acc, W2);
#pragma unroll
  for (int i = 0; i < 4; ++i) *(LAS f32x4*)(pp + i * 2048) = acc[i];
  BAR();
  {
    const int a = c.lane >> 2, gg = c.lane & 3;
    lptr rp = c.smem + LDS_CP + c.wave * 1024 + gg * 256 + ((a + 4 * gg) & 15) * 16;
    f32x4 y0 = *(LAS f32x4*)rp + *(LAS f32x4*)(rp + 16384);
    f32x4 y1 = *(LAS f32x4*)(rp + 8192) + *(LAS f32x4*)(rp + 16384 + 8192);
    v2u u0 = *(LAS v2u*)(c.smem + u_off + 1024 + c.tid * 8), u1 = *(LAS v2u*)(c.smem + u_off + U_STR + 1024 + c.tid * 8);
    unsigned dkp = *(LAS unsigned*)(c.smem + dk_off);
    float dk0 = bflo(dkp), dk1 = bfhi(dkp);
    float a0[4] = {bflo(u0.x), bfhi(u0.x), bflo(u0.y), bfhi(u0.y)}, a1[4] = {bflo(u1.x), bfhi(u1.x), bflo(u1.y), bfhi(u1.y)};
    v4u o;
#pragma unroll
    for (int i = 0; i < 4; ++i) {
      unsigned yb = pk2(y0[i], y1[i]), sk = pk2(a0[i] * dk0, a1[i] * dk1);
      o[i] = pk2(bflo(yb) + bflo(sk), bfhi(yb) + bfhi(sk));
    }
    __builtin_amdgcn_raw_buffer_store_b128(o, c.wsr, WS_ST + (chpair * 2048 + c.tid * 4) * 4, 0, SC1);
  }
  FENCE();
}
#define SV

constexpr int W2A = 15, W2B = 28;

__global__ __launch_bounds__(512, 2) void fused_kernel(
    const u16* __restrict__ xg, const u16* __restrict__ pAre, const u16* __restrict__ pAim, const u16* __restrict__ pBre,
    const u16* __restrict__ pBim, const u16* __restrict__ pCre, const u16* __restrict__ pCim, const u16* __restrict__ plogdt,
    const u16* __restrict__ pDsk, const u16* __restrict__ w1g, const u16* __restrict__ b1g, const u16* __restrict__ w2g,
    const u16* __restrict__ b2g, u16* __restrict__ outg, unsigned char* __restrict__ ws) {
  extern __shared__ __attribute__((aligned(16))) unsigned char smem_[];
  lptr smem = (lptr)smem_;
  unsigned long long probe_t = 0;
  const unsigned long long probe_0 = __builtin_amdgcn_s_memrealtime();
  asm volatile("" :: "s"(xg), "s"(pAre), "s"(pAim), "s"(pBre), "s"(pBim), "s"(pCre), "s"(pCim), "s"(plogdt), "s"(pDsk), "s"(w1g),
               "s"(b1g), "s"(w2g), "s"(b2g), "s"(outg), "s"(ws));
  const int tid = threadIdx.x, lane = tid & 63;
  const int wave = __builtin_amdgcn_readfirstlane(tid >> 6);
  const int bid = blockIdx.x, xcd = bid & 7, slot = bid >> 3;
  const bool aux = slot >= 28;
  const int l15 = lane & 15, g = lane >> 4;

  const rsrc_t wsr = __builtin_amdgcn_make_buffer_rsrc(ws, 0, WS_END, 0x00020000);
  const rsrc_t xr = __builtin_amdgcn_make_buffer_rsrc((void*)xg, 0, L_ * D_ * 2, 0x00020000);
  const v4u xd = mkdesc(xg, L_ * D_ * 2), w1d = mkdesc(w1g, D_ * F_ * 2), w2d = mkdesc(w2g, F_ * D_ * 2);

  const int tb = xcd * 32 + slot;
  const int ch0 = xcd * 80 + (aux ? 56 + (slot - 28) * 6 : slot * 2);
  const int nround = aux ? 3 : 1;
  const int gm = (xcd >> 1) * 4 + (slot & 3), gn = (xcd & 1) * 7 + (slot >> 2);
  const int mb = xcd * 4 + (slot & 3), nb = slot >> 2;

  unsigned epoch;
  asm volatile("global_load_dword %0, %1, %2 sc1" : "=v"(epoch) : "v"(0), "s"(ws) : "memory");
  {
    int vo = (tb * 8 + wave) * 1280 + lane * 16;
    glds(xd, smem + LDS_LN + wave * 1280, vo, 0);
    if (lane < 16) glds(xd, smem + LDS_LN + wave * 1280 + 1024, vo, 1024);
  }
  const int par_str = aux ? 1536 : 512;
  lptr par = smem + (aux ? LDS_PAR_AUX : LDS_PAR_MAIN);
  if (wave < 6) {
    const u16* src = wave == 0 ? pAre : wave == 1 ? pAim : wave == 2 ? pBre : wave == 3 ? pBim : wave == 4 ? pCre : pCim;
    const v4u pr = mkdesc(src, D_ * N_ * 2);
    int vo = ch0 * 256 + lane * 16;
    if (aux) glds(pr, par + wave * 1536 + 512, vo, 512);
    if (lane < 32) glds(pr, par + wave * par_str, vo, 0);
  } else if (wave == 6) {
    if (lane < 16) glds(mkdesc(b1g, F_ * 2), smem + LDS_B1, gn * 256 + lane * 16, 0);
    if (lane == 0) glds(mkdesc(plogdt, D_ * 2), smem + LDS_DT, ch0 * 2, 0);
  } else {
    if (lane < 10) glds(mkdesc(b2g, D_ * 2), smem + LDS_B2, nb * 160 + lane * 16, 0);
    if (lane == 0) glds(mkdesc(pDsk, D_ * 2), smem + LDS_DK, ch0 * 2, 0);
  }
  asm volatile("s_barrier" ::: "memory");
  const int rot = (((slot & 3) * 5 + 1) >> 1) + (((slot >> 2) * 3) >> 1);
  int xvo[2], wvo[2];
#pragma unroll
  for (int j = 0; j < 2; ++j) {
    int ins = wave * 2 + j;
    int r = ins * 8 + (lane >> 3);
    xvo[j] = (gm * 128 + r) * 1280 + (((lane & 7) ^ ((r >> 1) & 7)) << 4);
    int k = ins * 4 + g;
    wvo[j] = k * 3456 + gn * 256 + ((l15 ^ (((k & 3) << 2) | ((k >> 2) & 3))) << 4);
  }
  auto issue1 = [&](int i, int q) {
    int kc = i + rot;
    kc = kc >= 20 ? kc - 20 : kc >= 10 ? kc - 10 : kc;
    lptr st = smem + LDS_RING + (i % 3) * 32768 + wave * 2048;
    if (q < 2) glds(w1d, st + 16384 + q * 1024, wvo[q], kc * (64 * 3456));
    else glds(xd, st + (q - 2) * 1024, xvo[q - 2], kc * 128);
  };
  const int ax = xcd * 4 + (slot - 28);
  auto issue_pt = [&](int i, int q) {
    int lb = wave + 8 * i;
    int blk = ax * 17 + lb;
    blk = blk < 540 ? blk : 539;
    int s = blk / 10, nq = blk - s * 10;
    glds(w2d, smem + lb * 4096 + q * 1024, (lane >> 3) * 1280 + (lane & 7) * 16, (s * 32 + q * 8) * 1280 + nq * 128);
  };
  auto issue_any = [&](int i, int q0, int q1) {
    if (!aux) { for (int q = q0; q < q1; ++q) issue1(i, q); }
    else { for (int q = q0; q < q1; ++q) issue_pt(i, q); }
  };
  issue_any(0, 0, 4);
  if (tid == 0) *(LAS unsigned*)(smem + LDS_CNT) = 0u;
  if (tid < 16) *(LAS unsigned*)(smem + LDS_KD + (tid >> 3) * KD_STR + (tid & 7) * 4) = 0u;
  STAMPV(1);

  asm volatile("s_waitcnt vmcnt(4)\n\ts_barrier" : "+v"(epoch) :: "memory");
  const unsigned tag = __builtin_amdgcn_readfirstlane(epoch) + 1u;
  STAMPV(2);
  issue_any(1, 0, 2);

  float alpha, beta;
  {
    lptr rp = smem + LDS_LN + wave * 1280;
    v2u a = *(LAS v2u*)(rp + lane * 8), b = *(LAS v2u*)(rp + 512 + lane * 8);
    unsigned c = *(LAS unsigned*)(rp + 1024 + lane * 4);
    f32x2 v[5] = {{bflo(a.x), bfhi(a.x)}, {bflo(a.y), bfhi(a.y)}, {bflo(b.x), bfhi(b.x)}, {bflo(b.y), bfhi(b.y)}, {bflo(c), bfhi(c)}};
    const float sh = bflo(__builtin_amdgcn_readfirstlane(a.x));
    f32x2 s = {0.f, 0.f}, q = {0.f, 0.f};
#pragma unroll
    for (int i = 0; i < 5; ++i) { f32x2 d = v[i] - sh; s += d; q = __builtin_elementwise_fma(d, d, q); }
    float s1 = s.x + s.y, q1 = q.x + q.y;
    wave_sum2(s1, q1);
    float md = s1 * (1.f / D_);
    float var = fmaxf(q1 * (1.f / D_) - md * md, 0.f);
    float m1 = rbf(md + sh), den1 = rbf(__builtin_amdgcn_sqrtf(rbf(rbf(var) + 1e-5f)));
    float r1 = __builtin_amdgcn_rcpf(den1);
    unsigned o[5];
    s = (f32x2){0.f, 0.f}; q = (f32x2){0.f, 0.f};
#pragma unroll
    for (int i = 0; i < 5; ++i) {
      unsigned d = pk2(v[i].x - m1, v[i].y - m1);
      o[i] = pk2(bflo(d) * r1, bfhi(d) * r1);
      f32x2 w = {bflo(o[i]), bfhi(o[i])};
      s += w; q = __builtin_elementwise_fma(w, w, q);
    }
    *(LAS v2u*)(rp + lane * 8) = (v2u){o[0], o[1]};
    *(LAS v2u*)(rp + 512 + lane * 8) = (v2u){o[2], o[3]};
    *(LAS unsigned*)(rp + 1024 + lane * 4) = o[4];
    float s2 = s.x + s.y, q2 = q.x + q.y;
    wave_sum2(s2, q2);
    float mm = s2 * (1.f / D_);
    float var2 = fmaxf(q2 * (1.f / D_) - mm * mm, 0.f);
    float m2 = rbf(mm), den2 = rbf(__builtin_amdgcn_sqrtf(rbf(rbf(var2) + 1e-5f)));
    float r2 = __builtin_amdgcn_rcpf(den2);
    alpha = r1 * r2; beta = (m1 * r1 + m2) * r2;
  }
  STAMPV(3);
  issue_any(1, 2, 4);
  STAMPV(4);

#pragma unroll 1
  for (int rd = 0; rd < nround; ++rd) {
    const int c = wave >> 2;
    {
      const int half = (wave >> 1) & 1, part = wave & 1;
      const int n = half * 64 + lane;
      lptr pp = par + (rd * 2 + c) * 256 + n * 2;
      float dt = rbf(__builtin_amdgcn_exp2f(bflo(*(LAS u16*)(smem + LDS_DT + (rd * 2 + c) * 2)) * 1.44269504088896f));
      float a2 = dt * bflo(*(LAS u16*)pp) * 1.44269504088896f;
      float bt = dt * bflo(*(LAS u16*)(pp + par_str)) * 0.15915494309189535f;
      lptr dst = smem + LDS_KS + c * KCH + n * 4;
      if (part == 0) {
        dst += 21 * KROW;
        f32x2 V[8], G[4];
        V[0] = (f32x2){1.f, 0.f};
        V[1] = cexp_l(a2, bt, 8.f);
        G[1] = cexp_l(a2, bt, 64.f);
        V[2] = cmul(V[1], V[1]); V[3] = cmul(V[2], V[1]); V[4] = cmul(V[2], V[2]);
        V[5] = cmul(V[4], V[1]); V[6] = cmul(V[4], V[2]); V[7] = cmul(V[5], V[2]);
        G[2] = cmul(G[1], G[1]); G[3] = cmul(G[2], G[1]);
#pragma unroll
        for (int j = 0; j < 8; ++j) *(volatile LAS unsigned*)(dst + j * KROW) = pk2(V[j].x, V[j].y);
#pragma unroll
        for (int i = 1; i < 4; ++i) {
          f32x2 t[8];
#pragma unroll
          for (int j = 0; j < 8; ++j) t[j] = j == 0 ? G[i] : cmul(V[j], G[i]);
#pragma unroll
          for (int j = 0; j < 8; ++j) *(volatile LAS unsigned*)(dst + (i * 8 + j) * KROW) = pk2(t[j].x, t[j].y);
        }
      } else {
        f32x2 Bv = {dt * bflo(*(LAS u16*)(pp + 2 * par_str)), -dt * bflo(*(LAS u16*)(pp + 3 * par_str))};
        f32x2 Cv = {bflo(*(LAS u16*)(pp + 4 * par_str)), -bflo(*(LAS u16*)(pp + 5 * par_str))};
        f32x2 W[21];
        W[0] = cmul(Cv, Bv);
        f32x2 E1 = cexp_l(a2, -bt, 1.f), H = cexp_l(a2, -bt, 256.f);
        f32x2 E2 = cmul(E1, E1), E4 = cmul(E2, E2), H2 = cmul(H, H), H4 = cmul(H2, H2);
        W[1] = cmul(W[0], E1); W[2] = cmul(W[0], E2); W[3] = cmul(W[1], E2);
        W[4] = cmul(W[0], E4); W[5] = cmul(W[1], E4); W[6] = cmul(W[2], E4); W[7] = cmul(W[3], E4);
        W[8] = cmul(W[0], H); W[9] = cmul(W[2], H); W[10] = cmul(W[4], H); W[11] = cmul(W[6], H);
        W[12] = cmul(W[0], H2); W[13] = cmul(W[4], H2); W[14] = cmul(W[8], H2); W[15] = cmul(W[10], H2);
        W[16] = cmul(W[0], H4); W[17] = cmul(W[8], H4); W[18] = cmul(W[12], H4); W[19] = cmul(W[14], H4);
        W[20] = cmul(W[16], H4);
#pragma unroll
        for (int j = 0; j < 21; ++j) *(volatile LAS unsigned*)(dst + j * KROW) = pk2(W[j].x, W[j].y);
      }
    }
    if (rd == 0) STAMPV(5);
    BAR();
    if (rd == 0) STAMPV(6);
    if (rd == 0) {
      if (lane == 0)
        __builtin_amdgcn_raw_buffer_store_b64((v2u){__float_as_uint(alpha), __float_as_uint(beta)}, wsr, WS_STATS + (tb * 8 + wave) * 8, 0, SC1);
      for (int grp = wave; grp < 10; grp += 8) {
        lptr tp = smem + LDS_LN + (l15 >> 2) * 1280 + (grp * 64 + g * 16 + (lane & 3) * 4) * 2;
        v2u lo = trread(tp), hi = trread(tp + 4 * 1280);
        __builtin_amdgcn_raw_buffer_store_b128((v4u){lo.x, lo.y, hi.x, hi.y}, wsr, WS_XNB + (tb * 640 + grp * 64 + lane) * 16, 0, SC1);
      }
      FENCE();
    }
    if (rd == 0) STAMPV(7);
    {
      const int rt = (wave >> 1) & 1, ct = wave & 1;
      int ur = rt * 16 + l15; ur = ur < 21 ? ur : 20;
      lptr ap = smem + LDS_KS + c * KCH + ur * KROW + g * 16;
      lptr bp = smem + LDS_KS + c * KCH + (21 + ct * 16 + l15) * KROW + g * 16;
      v4u fa[8], fb[8];
#pragma unroll
      for (int ks = 0; ks < 8; ++ks) { fa[ks] = *(LAS v4u*)(ap + ks * 64); fb[ks] = *(LAS v4u*)(bp + ks * 64); }
      SB();
      f32x4 d = {0.f, 0.f, 0.f, 0.f};
#pragma unroll
      for (int ks = 0; ks < 8; ++ks) d = mfma(fa[ks], fb[ks], d);
      LAS u16* kd = (LAS u16*)(smem + LDS_KD + c * KD_STR + 32);
      const int j = ct * 16 + l15;
      unsigned p01 = pk2(d.x, d.y), p23 = pk2(d.z, d.w);
      if (rt == 0) {
        int i0 = g < 2 ? g * 4 + 8 * j : g == 2 ? 256 + 4 * j : 384 + 2 * j;
        int i2 = g < 3 ? i0 + 2 : i0 + 64;
        *(LAS unsigned*)(kd + i0) = p01;
        *(LAS unsigned*)(kd + i2) = p23;
      } else if (g == 0) {
        kd[512 + j] = (u16)p01; kd[544 + j] = (u16)(p01 >> 16); kd[576 + j] = (u16)p23; kd[608 + j] = (u16)(p23 >> 16);
      } else if (g == 1 && j == 0) {
        kd[640] = (u16)p01;
      }
    }
    if (rd == 0) STAMPV(8);
    if (aux && rd == 0) {
      if (wave == 0) { for (int q = 0; q < 4; ++q) issue_pt(2, q); }
      WAITV(0);
#pragma unroll 1
      for (int i = 0; i < 3; ++i) {
        int lb = wave + 8 * i;
        int blk = ax * 17 + lb;
        if (lb < 17 && blk < 540) {
          int s = blk / 10, nq = blk - s * 10;
#pragma unroll
          for (int cc = 0; cc < 4; ++cc) {
            lptr tp = smem + lb * 4096 + (4 * g + (l15 >> 2)) * 128 + cc * 32 + (lane & 3) * 8;
            v2u lo = trread(tp), hi = trread(tp + 16 * 128);
            int n16 = nq * 4 + cc, nbb = n16 / 5, j = n16 - nbb * 5;
            __builtin_amdgcn_raw_buffer_store_b128((v4u){lo.x, lo.y, hi.x, hi.y}, wsr, WS_W2T + lane * 16 + ((nbb * KS2 + s) * 5 + j) * 1024, 0, SC1);
          }
        }
      }
      FENCE();
    }
    if (aux && rd == 1) WAITV(0);
    BAR();
    if (rd == 0) STAMPV(9);
    if (aux) {
      if (tid == 0 && rd < 2) __builtin_amdgcn_raw_buffer_store_b32(tag, wsr, rd == 0 ? WS_F1 + bid * 4 : WS_F2 + ax * 4, 0, SC1);
    } else {
      issue1(2, 0); issue1(2, 1); issue1(2, 2); issue1(2, 3);
    }
    if (rd == 0) STAMPV(10);
    {
      lptr kc = smem + (rd + 1 == nround ? LDS_KCL : rd * KC_SET) + c * 4 * KC_STR;
      const int q = tid & 255;
      const int l0 = 2047 - 8 * q;
      const int sg = l0 < 256 ? 256 - l0 : 0;
      const float lf = (float)(l0 + sg);
      lptr kb = smem + LDS_KD + c * KD_STR + 32 + 2 * (128 - 0x4300 - sg);
      unsigned P[6];
#pragma unroll
      for (int k = 0; k < 6; ++k) {
        unsigned r = pk2(lf - (float)(2 * k), lf - (float)(2 * k + 1));
        u16x2 v;
        v.x = *(LAS u16*)(kb + 2 * (r & 0xffffu));
        v.y = k < 5 ? *(LAS u16*)(kb + 2 * (r >> 16)) : (u16)0;
        P[k] = __builtin_bit_cast(unsigned, v);
      }
      unsigned Q[5];
#pragma unroll
      for (int k = 0; k < 5; ++k) Q[k] = __builtin_amdgcn_alignbit(P[k + 1], P[k], 16);
      *(LAS v4u*)(kc + 0 * KC_STR + q * 16) = (v4u){Q[1], Q[2], Q[3], Q[4]};
      *(LAS v4u*)(kc + 1 * KC_STR + q * 16) = (v4u){P[1], P[2], P[3], P[4]};
      *(LAS v4u*)(kc + 2 * KC_STR + q * 16) = (v4u){Q[0], Q[1], Q[2], Q[3]};
      *(LAS v4u*)(kc + 3 * KC_STR + q * 16) = (v4u){P[0], P[1], P[2], P[3]};
      if (q < 8) *(LAS v4u*)(kc + (q >> 1) * KC_STR + 4096 + (q & 1) * 16) = (v4u){0u, 0u, 0u, 0u};
    }
    if (rd == 0) STAMPV(11);
  }
  if (tid < 256) *(LAS v4u*)(smem + LDS_U + (tid >> 6) * U_STR + (tid & 63) * 16) = (v4u){0u, 0u, 0u, 0u};
  STAMPV(12);

  const int s0 = wave * 7 - (wave > 6 ? 1 : 0);
  Ctx cx;
  cx.smem = smem; cx.wsr = wsr; cx.tid = tid; cx.lane = lane; cx.wave = wave; cx.l15 = l15; cx.g = g; cx.tag = tag;
  cx.w2off = (nb * KS2 + s0) * 5120;
  cx.w2last = (nb * KS2 + (s0 + 6 < KS2 ? s0 + 6 : KS2 - 1)) * 5120;
  v4u W2[35];
  const int ugo = WS_XNB + (((wave & 3) * 64 + lane) * 640 + ch0 + (wave >> 2)) * 16;
  const int uwo = (wave >> 2) * U_STR + 1024 + ((wave & 3) * 64 + lane) * 16;

  if (!aux) {
    const int fg = wave >> 1, th = wave & 1;
    lptr xb[2];
    lptr wb[2][2][2];
#pragma unroll
    for (int ks = 0; ks < 2; ++ks) xb[ks] = smem + LDS_RING + (th * 64 + l15) * 128 + (((4 * (ks ^ th) + g) ^ (l15 >> 1)) << 4);
#pragma unroll
    for (int ft = 0; ft < 2; ++ft)
#pragma unroll
      for (int tp = 0; tp < 2; ++tp) {
        int q = l15 >> 2, p = lane & 3;
        int row = 8 * g + 4 * tp + q;
        int chn = 4 * fg + 2 * ft + (p >> 1);
#pragma unroll
        for (int ks = 0; ks < 2; ++ks)
          wb[ks][ft][tp] = smem + LDS_RING + 16384 + (ks ^ th) * 8192 + 256 * row + ((chn ^ ((q << 2) | ((2 * g + tp) & 3))) << 4) + 8 * (p & 1);
      }
    f32x4 acc1[2][4], cs[2];
#pragma unroll
    for (int a = 0; a < 2; ++a) {
      cs[a] = (f32x4){0.f, 0.f, 0.f, 0.f};
#pragma unroll
      for (int b = 0; b < 4; ++b) acc1[a][b] = (f32x4){0.f, 0.f, 0.f, 0.f};
    }
    const v4u ones = {0x3f803f80u, 0x3f803f80u, 0x3f803f80u, 0x3f803f80u};
    v4u XA[2][2][4], WA[2][2][2];
    auto rdw = [&](int i, int ks) {
      const int so = (i % 3) * 32768, bf = i & 1;
#pragma unroll
      for (int ft = 0; ft < 2; ++ft) {
        v2u lo = trread(wb[ks][ft][0] + so), hi = trread(wb[ks][ft][1] + so);
        WA[bf][ks][ft] = (v4u){lo.x, lo.y, hi.x, hi.y};
      }
    };
    auto rdx = [&](int i, int ks) {
      const int so = (i % 3) * 32768, bf = i & 1;
#pragma unroll
      for (int tt = 0; tt < 4; ++tt) XA[bf][ks][tt] = *(LAS v4u*)(xb[ks] + so + tt * 2048);
    };
    WAITV(4);
    BAR();
    if (tid == 0) __builtin_amdgcn_raw_buffer_store_b32(tag, wsr, WS_F1 + bid * 4, 0, SC1);
    STAMPV(13);
    rdw(0, 0); rdx(0, 0); rdw(0, 1); rdx(0, 1);
    v4u fl;
    v2u st[4];
    v4u ug;
#pragma unroll
    for (int i = 0; i < 10; ++i) {
      const int bf = i & 1;
      if (i + 1 < 10) {
        if (i == 0) { }
        else if (i == 1) { if (wave == 0) WAITV(5); else WAITV(4); }
        else if (i + 2 < 10) WAITV(4);
        else {
          asm volatile("s_waitcnt vmcnt(0)" : "+v"(fl) :: "memory");
          if (wave < 2) { if (!flags_ok(fl, wave == 0 ? 256 : 32, tag, lane)) poll_flags(wsr, WS_F1 + wave * 1024, wave == 0 ? 256 : 32, tag, lane); }
        }
        BAR();
      }
      SB();
#pragma unroll
      for (int q = 0; q < 4; ++q) {
        const int ks = q >> 1, ft = q & 1;
        if (i + 1 < 10) { if (ft == 0) rdw(i + 1, ks); else rdx(i + 1, ks); }
        SB();
#pragma unroll
        for (int tt = 0; tt < 4; ++tt) acc1[ft][tt] = mfma(WA[bf][ks][ft], XA[bf][ks][tt], acc1[ft][tt]);
        if (ks == 0) cs[ft] = mfma(WA[bf][0][ft], ones, cs[ft]);
        SB();
        if (i + 3 < 10) issue1(i + 3, q);
        if (i == 6 && q == 3) asm volatile("buffer_load_dwordx4 %0, %1, %2, 0 offen sc1" : "=v"(fl) : "v"(WS_F1 + (wave & 1) * 1024 + lane * 16), "s"(mkdesc(ws, WS_END)) : "memory");
        if (i == 8) {
          st[q] = __builtin_amdgcn_raw_buffer_load_b64(wsr, WS_STATS + (gm * 128 + th * 64 + q * 16 + l15) * 8, 0, SC1);
          if (q == 3) ug = __builtin_amdgcn_raw_buffer_load_b128(wsr, ugo, 0, SC1);
        }
        if (i == 9) {
          if (q == 0) ldw2r<0, 1>(cx, W2);
          if (q == 1) ldw2r<1, 2>(cx, W2);
          if (q == 2) ldw2r<2, 3>(cx, W2);
        }
        SB();
      }
      STAMPV(14 + i);
    }
    if (l15 == 0) {
#pragma unroll
      for (int ft = 0; ft < 2; ++ft) *(LAS f32x4*)(smem + LDS_CS + ((wave * 2 + ft) * 4 + g) * 16) = cs[ft];
    }
    BAR();
    STAMPV(26);
    const int sidx = gn * 4 + fg;
    const int hoff = sidx < KS2 ? WS_HT + lane * 16 + ((gm * 8 + th * 4) * KS2 + sidx) * 1024 : WS_END;
    f32x4 csum[2], bb[2];
#pragma unroll
    for (int ft = 0; ft < 2; ++ft) {
      v2u b1v = *(LAS v2u*)(smem + LDS_B1 + (fg * 32 + ft * 16 + g * 4) * 2);
      bb[ft] = (f32x4){bflo(b1v.x), bfhi(b1v.x), bflo(b1v.y), bfhi(b1v.y)};
      csum[ft] = *(LAS f32x4*)(smem + LDS_CS + (((fg * 2) * 2 + ft) * 4 + g) * 16) + *(LAS f32x4*)(smem + LDS_CS + (((fg * 2 + 1) * 2 + ft) * 4 + g) * 16);
    }
#pragma unroll
    for (int tt = 0; tt < 4; ++tt) {
      float al = __uint_as_float(st[tt].x), be = __uint_as_float(st[tt].y);
      v4u hf;
#pragma unroll
      for (int ft = 0; ft < 2; ++ft) {
        f32x4 z = acc1[ft][tt] * al + (bb[ft] - csum[ft] * be);
        f32x4 ar = z * (z * z * (-0.044715f * 2.302208198f) + (-2.302208198f));
        f32x4 o;
#pragma unroll
        for (int e = 0; e < 4; ++e) o[e] = z[e] * __builtin_amdgcn_rcpf(1.f + __builtin_amdgcn_exp2f(ar[e]));
        hf[2 * ft] = pk2(o.x, o.y);
        hf[2 * ft + 1] = pk2(o.z, o.w);
        SB();
        if (tt == 0 && ft == 0) ldw2r<3, 5>(cx, W2);
        if (tt == 0 && ft == 1) ldw2r<5, 6>(cx, W2);
        if (tt == 1 && ft == 0) ldw2r<6, 8>(cx, W2);
        if (tt == 1 && ft == 1) ldw2r<8, 9>(cx, W2);
        if (tt == 2 && ft == 0) ldw2r<9, 11>(cx, W2);
        if (tt == 2 && ft == 1) ldw2r<11, 12>(cx, W2);
        if (tt == 3 && ft == 0) ldw2r<12, 14>(cx, W2);
        if (tt == 3 && ft == 1) ldw2r<14, W2A>(cx, W2);
        SB();
      }
      __builtin_amdgcn_raw_buffer_store_b128(hf, wsr, hoff + tt * (KS2 * 1024), 0, SC1);
      SB();
    }
    *(LAS v4u*)(smem + LDS_U + uwo) = ug;
    STAMPV(28);
  } else {
    if (wave == 0) poll_flags(wsr, WS_F1, 256, tag, lane);
    BAR();
    STAMPV(13);
    v4u ug0 = __builtin_amdgcn_raw_buffer_load_b128(wsr, ugo, 0, SC1);
    v4u ug1 = __builtin_amdgcn_raw_buffer_load_b128(wsr, ugo + 32, 0, SC1);
    v4u ug2 = __builtin_amdgcn_raw_buffer_load_b128(wsr, ugo + 64, 0, SC1);
    *(LAS v4u*)(smem + LDS_U + uwo) = ug0;
    *(LAS v4u*)(smem + LDS_U + 2 * U_STR + uwo) = ug1;
    BAR();
    STAMPV(14);
    conv_round<0, 0, false>(cx, 0, LDS_U, (ch0 >> 1), LDS_DK, -1, W2 SV);
    if (wave == 0) poll_flags(wsr, WS_F2, 32, tag, lane);
    BAR();
    STAMPV(15);
    *(LAS v4u*)(smem + LDS_U + uwo) = ug2;
    conv_round<0, W2A, false>(cx, KC_SET, LDS_U + 2 * U_STR, (ch0 >> 1) + 1, LDS_DK + 4, -1, W2 SV);
    STAMPV(28);
  }
  if (bid == 0 && tid == 64) __builtin_amdgcn_raw_buffer_store_b32(tag, wsr, WS_EPOCH, 0, SC1);
  BAR();
  STAMPV(33);

  conv_round<W2A, W2B, true>(cx, LDS_KCL, LDS_U, (ch0 >> 1) + nround - 1, LDS_DK + (nround - 1) * 4, aux ? -1 : WS_FH + (gm * 16 + gn) * 4, W2 SV);
  STAMPV(36);

  {
    v4u fh = __builtin_amdgcn_raw_buffer_load_b128(wsr, WS_FH + (mb >> 1) * 64 + (lane & 3) * 16, 0, SC1);
    FENCE();
    ldw2r<W2B, 35>(cx, W2);
    FENCE();
    if (!flags_ok(fh, 14, tag, lane & 3)) poll_flags(wsr, WS_FH + (mb >> 1) * 64, 14, tag, lane);
    FENCE();
    STAMPV(45);
    v4u H[2][7];
    const int hb = (mb * 4 * KS2 + s0) * 1024, hl = s0 + 6 < KS2 ? 6144 : 5120;
    auto ldh = [&](int r, int s) {
      H[r & 1][s] = __builtin_amdgcn_raw_buffer_load_b128(wsr, WS_HT + lane * 16, hb + r * (KS2 * 1024) + (s < 6 ? s * 1024 : hl), SC1);
    };
#pragma unroll
    for (int s = 0; s < 7; ++s) ldh(0, s);
#pragma unroll
    for (int s = 0; s < 7; ++s) ldh(1, s);
    FENCE();
    STAMPV(46);
    const int ft_ = lane >> 2, fg_ = lane & 3;
    const int jj = wave < 4 ? wave : 4;
    const int rdo = fg_ * 256 + ((ft_ + 4 * fg_) & 15) * 16;
    const int wro = g * 256 + ((l15 + 4 * g) & 15) * 16;
    v2u b2v = *(LAS v2u*)(smem + LDS_B2 + (jj * 16 + fg_ * 4) * 2);
    auto gate = [&](lptr pb, int stride) -> v2u {
      f32x4 z = (*(LAS f32x4*)(pb) + *(LAS f32x4*)(pb + stride)) + (*(LAS f32x4*)(pb + 2 * stride) + *(LAS f32x4*)(pb + 3 * stride));
      z += (*(LAS f32x4*)(pb + 4 * stride) + *(LAS f32x4*)(pb + 5 * stride)) + (*(LAS f32x4*)(pb + 6 * stride) + *(LAS f32x4*)(pb + 7 * stride));
      unsigned z01 = pk2(z.x, z.y), z23 = pk2(z.z, z.w);
      z01 = pk2(bflo(z01) + bflo(b2v.x), bfhi(z01) + bfhi(b2v.x));
      z23 = pk2(bflo(z23) + bflo(b2v.y), bfhi(z23) + bfhi(b2v.y));
      float zz[4] = {bflo(z01), bfhi(z01), bflo(z23), bfhi(z23)};
#pragma unroll
      for (int e = 0; e < 4; ++e) zz[e] = zz[e] * __builtin_amdgcn_rcpf(1.f + __builtin_amdgcn_exp2f(zz[e] * -1.44269504088896f));
      return (v2u){pk2(zz[0], zz[1]), pk2(zz[2], zz[3])};
    };
    v2u gv[3], xv[4];
    v4u sreg[2], fs;
    const int rl = wave < 4 ? 3 : wave - 4;
    const int xo = ((mb * 64 + ft_) * 640 + nb * 80 + jj * 16 + fg_ * 4) * 2;
#pragma unroll
    for (int r = 0; r < 4; ++r) {
      if (r == 2) fs = __builtin_amdgcn_raw_buffer_load_b128(wsr, WS_FS + lane * 16, 0, SC1);
      if (r == 3) {
#pragma unroll
        for (int i = 0; i < 2; ++i) {
          int it = tid + 512 * i; it = it < 640 ? it : 639;
          sreg[i] = __builtin_amdgcn_raw_buffer_load_b128(wsr, WS_ST + ((nb * 40 + (it >> 4)) * 2048 + mb * 64) * 4 + (it & 15) * 16, 0, SC1);
        }
        xv[3] = __builtin_amdgcn_raw_buffer_load_b64(xr, xo + rl * (16 * 1280), 0, 0);
#pragma unroll
        for (int i = 0; i < 3; ++i) xv[i] = __builtin_amdgcn_raw_buffer_load_b64(xr, xo + i * (16 * 1280), 0, 0);
      }
      SB();
      if (r > 0 && wave < 4) gv[r - 1] = gate(smem + LDS_P2 + ((r - 1) & 1) * 32768 + wave * 1024 + rdo, 4096);
      SB();
      f32x4 acc[5];
#pragma unroll
      for (int j = 0; j < 5; ++j) acc[j] = (f32x4){0.f, 0.f, 0.f, 0.f};
#pragma unroll
      for (int s = 0; s < 6; ++s) {
#pragma unroll
        for (int j = 0; j < 5; ++j) acc[j] = mfma(W2[s * 5 + j], H[r & 1][s], acc[j]);
        SB();
        if (r + 2 < 4 && s > 0) ldh(r + 2, s - 1);
        SB();
      }
      if (wave < 6) {
#pragma unroll
        for (int j = 0; j < 5; ++j) acc[j] = mfma(W2[30 + j], H[r & 1][6], acc[j]);
      }
      SB();
      if (r + 2 < 4) { ldh(r + 2, 5); ldh(r + 2, 6); }
      STAMPV(47 + r * 3);
      if (r == 0) BAR();
#pragma unroll
      for (int j = 0; j < 4; ++j) *(LAS f32x4*)(smem + LDS_P2 + (r & 1) * 32768 + (wave * 4 + j) * 1024 + wro) = acc[j];
      *(LAS f32x4*)(smem + LDS_P4 + r * 8192 + wave * 1024 + wro) = acc[4];
      if (r == 0) WAITV(14);
      if (r == 2 && wave == 7) { if (!flags_ok(fs, 256, tag, lane)) poll_flags(wsr, WS_FS, 256, tag, lane); }
      if (r == 3) {
        *(LAS v4u*)(smem + LDS_S2 + (tid >> 4) * S2_STR + (tid & 15) * 16) = sreg[0];
        if (tid < 128) *(LAS v4u*)(smem + LDS_S2 + (32 + (tid >> 4)) * S2_STR + (tid & 15) * 16) = sreg[1];
      }
      STAMPV(48 + r * 3);
      BAR();
      STAMPV(49 + r * 3);
      if (r == 0 && tid == 0) __builtin_amdgcn_raw_buffer_store_b32(tag, wsr, WS_FS + bid * 4, 0, SC1);
    }
    auto emit = [&](int r, v2u gg, v2u xx) {
      lptr sp = smem + LDS_S2 + (jj * 8 + fg_ * 2) * S2_STR + (r * 16 + ft_) * 4;
      unsigned s01 = *(LAS unsigned*)sp, s23 = *(LAS unsigned*)(sp + S2_STR);
      unsigned p01 = pk2(bflo(s01) * bflo(gg.x), bfhi(s01) * bfhi(gg.x)), p23 = pk2(bflo(s23) * bflo(gg.y), bfhi(s23) * bfhi(gg.y));
      v2u o = {pk2(bflo(xx.x) + bflo(p01), bfhi(xx.x) + bfhi(p01)), pk2(bflo(xx.y) + bflo(p23), bfhi(xx.y) + bfhi(p23))};
      *(GAS v2u*)((GAS unsigned char*)outg + xo + r * (16 * 1280)) = o;
    };
    {
      v2u gl = gate(wave < 4 ? smem + LDS_P2 + 32768 + wave * 1024 + rdo : smem + LDS_P4 + rl * 8192 + rdo, wave < 4 ? 4096 : 1024);
      emit(rl, gl, xv[3]);
    }
    if (wave < 4) {
#pragma unroll
      for (int r = 0; r < 3; ++r) emit(r, gv[r], xv[r]);
    }
  }
  STAMPV(62);
  if (!aux) { unsigned long long d = probe_t - probe_0;  unsigned long long until = __builtin_amdgcn_s_memrealtime() + d; if (d < 20000) { while (__builtin_amdgcn_s_memrealtime() < until) __builtin_amdgcn_s_sleep(1); } }
}

}

extern "C" void kernel_launch(void* const* d_in, const int* in_sizes, int n_in, void* d_out, int out_size, void* d_ws,
                              size_t ws_size, hipStream_t stream) {
  using namespace fk;
  (void)hipFuncSetAttribute((const void*)fused_kernel, hipFuncAttributeMaxDynamicSharedMemorySize, LDS_TOTAL);
  fused_kernel<<<256, 512, LDS_TOTAL, stream>>>(
      (const u16*)d_in[0], (const u16*)d_in[1], (const u16*)d_in[2], (const u16*)d_in[3], (const u16*)d_in[4], (const u16*)d_in[5],
      (const u16*)d_in[6], (const u16*)d_in[7], (const u16*)d_in[8], (const u16*)d_in[10], (const u16*)d_in[11], (const u16*)d_in[12],
      (const u16*)d_in[13], (u16*)d_out, (unsigned char*)d_ws);
}
```

```cpp
#include <hip/hip_runtime.h>
#include <hip/hip_bf16.h>
#include <stdint.h>

namespace fk {

typedef unsigned short u16;
typedef unsigned v4u __attribute__((ext_vector_type(4)));
typedef unsigned v2u __attribute__((ext_vector_type(2)));
typedef unsigned v16u __attribute__((ext_vector_type(16)));
typedef unsigned v8u __attribute__((ext_vector_type(8)));
typedef unsigned v3u __attribute__((ext_vector_type(3)));
typedef float f32x3 __attribute__((ext_vector_type(3)));
typedef float f32x4 __attribute__((ext_vector_type(4)));
typedef float f32x2 __attribute__((ext_vector_type(2)));
typedef float f32x16 __attribute__((ext_vector_type(16)));
typedef __bf16 bf16x8 __attribute__((ext_vector_type(8)));
typedef __bf16 bf16x2 __attribute__((ext_vector_type(2)));
typedef short s16x4 __attribute__((ext_vector_type(4)));
#define LAS __attribute__((address_space(3)))
#define GAS __attribute__((address_space(1)))
#define CAS __attribute__((address_space(4)))
typedef __amdgpu_buffer_rsrc_t rsrc_t;
#define LD(T, a) (*(LAS T*)(unsigned)(a))
#define LDV(T, a) (*(volatile LAS T*)(unsigned)(a))

constexpr int L_ = 2048, D_ = 640, N_ = 128, F_ = 1728;
constexpr int KS2 = F_ / 32;

constexpr int WS_EPOCH = 0;
constexpr int WS_F1 = 1024;
constexpr int WS_F3 = WS_F1 + 64 * 128;
constexpr int WS_FH = WS_F3 + 32 * 128;
constexpr int WS_FS = WS_FH + 256 * 128;
constexpr int WS_STATS = WS_FS + 256 * 128;
constexpr int WS_CS = WS_STATS + L_ * 32;
constexpr int WS_XNB = WS_CS + 32768;
constexpr int WS_W2T = WS_XNB + L_ * D_ * 2;
constexpr int WS_HT = WS_W2T + 8 * KS2 * 5 * 1024;
constexpr int WS_ST = WS_HT + 128 * KS2 * 1024;
constexpr int WS_END = WS_ST + L_ * D_ * 2;

constexpr int LDS_ST = 98304;
constexpr int KD_STR = 1328;
constexpr int LDS_KD = LDS_ST;
constexpr int KROW = 544;
constexpr int KCH = 53 * KROW;
constexpr int LDS_KS = LDS_ST + 6144;
constexpr int LDS_LN_MAIN = LDS_ST + 20480, LDS_RS = LDS_LN_MAIN + 10240, LDS_PAR_MAIN = LDS_RS + 256;
constexpr int LDS_LN_AUX = 57856, LDS_PAR_AUX = LDS_LN_AUX + 10240, LDS_CSP = 90112, LDS_KS_AUX = 0;
constexpr int LDS_B1 = LDS_KS + 2 * KCH;
constexpr int LDS_B2 = LDS_B1 + 256, LDS_DK = LDS_B2 + 256, LDS_CNT = LDS_DK + 16, LDS_EP = LDS_CNT + 16, LDS_DUMMY = LDS_EP + 16;
constexpr int LDS_TOTAL = 163840;
static_assert(LDS_DUMMY + 16 <= LDS_TOTAL && 4 * KD_STR <= 6144, "lds");
constexpr int KC_STR = 4160;
constexpr int KC_SET = 8 * KC_STR;
constexpr int LDS_KCA = 0;
constexpr int LDS_KCL = LDS_KS;
constexpr int U_STR = 4608;
constexpr int LDS_U = LDS_KCL + KC_SET;
constexpr int LDS_CP = KC_SET;
static_assert(LDS_U + 4 * U_STR <= LDS_B1 && LDS_CP + 65536 <= LDS_KCL, "lds");
constexpr int LDS_P4 = LDS_KCL;
constexpr int LDS_P2 = 0;
constexpr int LDS_S2 = 65536;
constexpr int S2_STR = 272;

__device__ __forceinline__ float bflo(unsigned v) { return __uint_as_float(v << 16); }
__device__ __forceinline__ float bfhi(unsigned v) { return __uint_as_float(v & 0xffff0000u); }
__device__ __forceinline__ f32x2 unpk(unsigned v) { return (f32x2){bflo(v), bfhi(v)}; }
__device__ __forceinline__ unsigned pk2(float a, float b) {
  f32x2 v = {a, b};
  return __builtin_bit_cast(unsigned, __builtin_convertvector(v, bf16x2));
}
__device__ __forceinline__ float rbf(float a) { return bflo(pk2(a, a)); }
__device__ __forceinline__ f32x2 fma2(f32x2 a, f32x2 b, f32x2 c) { return __builtin_elementwise_fma(a, b, c); }
__device__ __forceinline__ float dot2(unsigned a, unsigned b, float c) {
  return __builtin_amdgcn_fdot2_f32_bf16(__builtin_bit_cast(bf16x2, a), __builtin_bit_cast(bf16x2, b), c, false);
}
__device__ __forceinline__ f32x4 mfma(v4u a, v4u b, f32x4 c) {
  return __builtin_amdgcn_mfma_f32_16x16x32_bf16(__builtin_bit_cast(bf16x8, a), __builtin_bit_cast(bf16x8, b), c, 0, 0, 0);
}
__device__ __forceinline__ void mfma_ip(f32x4& c, v4u a, v4u b) {
  asm volatile("v_mfma_f32_16x16x32_bf16 %0, %1, %2, %0" : "+v"(c) : "v"(a), "v"(b));
}
__device__ __forceinline__ void mfma32_ip(f32x16& c, v4u a, v4u b) {
  asm volatile("v_mfma_f32_32x32x16_bf16 %0, %1, %2, %0" : "+v"(c) : "v"(a), "v"(b));
}
__device__ __forceinline__ void mfma32_z(f32x16& c, v4u a, v4u b) {
  asm volatile("v_mfma_f32_32x32x16_bf16 %0, %1, %2, 0" : "=&v"(c) : "v"(a), "v"(b));
}
__device__ __forceinline__ void mfma_z(f32x4& c, v4u a, v4u b) {
  asm volatile("v_mfma_f32_16x16x32_bf16 %0, %1, %2, 0" : "=&v"(c) : "v"(a), "v"(b));
}
__device__ __forceinline__ float vmul(float a, float b) { float r; asm volatile("v_mul_f32 %0, %1, %2" : "=v"(r) : "v"(a), "v"(b)); return r; }
__device__ __forceinline__ float vadd(float a, float b) { float r; asm volatile("v_add_f32 %0, %1, %2" : "=v"(r) : "v"(a), "v"(b)); return r; }
__device__ __forceinline__ float vfma(float a, float b, float c) { float r; asm volatile("v_fma_f32 %0, %1, %2, %3" : "=v"(r) : "v"(a), "v"(b), "v"(c)); return r; }
__device__ __forceinline__ float vfnma(float a, float b, float c) { float r; asm volatile("v_fma_f32 %0, -%1, %2, %3" : "=v"(r) : "v"(a), "v"(b), "v"(c)); return r; }
__device__ __forceinline__ f32x2 vadd2(f32x2 a, f32x2 b) {
  float x, y;
  asm volatile("v_add_f32 %0, %2, %4\n\tv_add_f32 %1, %3, %5" : "=&v"(x), "=v"(y) : "v"(a.x), "v"(a.y), "v"(b.x), "v"(b.y));
  return (f32x2){x, y};
}
struct cf { float x, y; };
__device__ __forceinline__ cf cmul4(cf a, cf e) {
  cf t;
  asm volatile("v_mul_f32 %0, %2, %4\n\tv_mul_f32 %1, %2, %5\n\tv_fma_f32 %0, -%3, %5, %0\n\tv_fma_f32 %1, %3, %4, %1"
               : "=&v"(t.x), "=&v"(t.y) : "v"(a.x), "v"(a.y), "v"(e.x), "v"(e.y));
  return t;
}
__device__ __forceinline__ cf cmul5(cf a, cf e, unsigned& p) {
  cf t;
  asm volatile("v_mul_f32 %0, %3, %5\n\tv_mul_f32 %1, %3, %6\n\tv_fma_f32 %0, -%4, %6, %0\n\tv_fma_f32 %1, %4, %5, %1\n\tv_cvt_pk_bf16_f32 %2, %0, %1"
               : "=&v"(t.x), "=&v"(t.y), "=v"(p) : "v"(a.x), "v"(a.y), "v"(e.x), "v"(e.y));
  return t;
}
template <int V> struct ic { static constexpr int v = V; };
template <int N, int I = 0, class F>
__device__ __forceinline__ void static_for(F&& f) { if constexpr (I < N) { f(ic<I>{}); static_for<N, I + 1>(f); } }
__device__ __forceinline__ v2u trread(int a) {
  return __builtin_bit_cast(v2u, __builtin_amdgcn_ds_read_tr16_b64_v4i16((LAS s16x4*)(unsigned)a));
}
__device__ __forceinline__ rsrc_t as_rsrc(v4u d) {
  return __builtin_amdgcn_make_buffer_rsrc((void*)(((unsigned long long)d.y << 32) | d.x), 0, d.z, 0x00020000);
}
__device__ __forceinline__ void glds(v4u desc, int lds, int voff, int soff) {
  asm volatile("s_mov_b32 m0, %0\n\ts_nop 0\n\tbuffer_load_dwordx4 %1, %2, %3 offen lds" :: "s"(lds), "v"(voff), "s"(desc), "s"(soff) : "memory");
}
__device__ __forceinline__ void glds_m(v4u desc, int lds, int voff, int soff, unsigned long long mask) {
  asm volatile("s_mov_b32 m0, %0\n\ts_mov_b64 exec, %4\n\tbuffer_load_dwordx4 %1, %2, %3 offen lds\n\ts_mov_b64 exec, -1"
               :: "s"(lds), "v"(voff), "s"(desc), "s"(soff), "s"(mask) : "memory");
}
__device__ __forceinline__ void glds_s(v4u desc, int lds, int voff, int soff, unsigned long long mask) {
  asm volatile("s_mov_b32 m0, %0\n\ts_mov_b64 exec, %4\n\tbuffer_load_dwordx4 %1, %2, %3 offen sc1 lds\n\ts_mov_b64 exec, -1"
               :: "s"(lds), "v"(voff), "s"(desc), "s"(soff), "s"(mask) : "memory");
}
__device__ __forceinline__ void touch3(int tx, int tw, v4u xd, v4u wd, int sx, int sw) {
  asm volatile("buffer_load_dword v255, %0, %2, %4 offen\n\tbuffer_load_dword v255, %1, %3, %5 offen\n\tbuffer_load_dword v255, %1, %3, %6 offen"
               :: "v"(tx), "v"(tw), "s"(xd), "s"(wd), "s"(sx), "s"(sw), "s"(sw + 4 * 3456) : "memory", "v255");
}
template <int C> __device__ __forceinline__ void add_m0(int base) { asm volatile("s_add_i32 m0, %0, %c1" :: "s"(base), "i"(C) : "memory", "scc"); }
__device__ __forceinline__ void set_m0(int lds) { asm volatile("s_mov_b32 m0, %0\n\ts_nop 0" :: "s"(lds) : "memory"); }
template <int OFF>
__device__ __forceinline__ void glds_o(v4u desc, int voff, int soff) {
  asm volatile("buffer_load_dwordx4 %0, %1, %2 offen offset:%c3 lds" :: "v"(voff), "s"(desc), "s"(soff), "i"(OFF) : "memory");
}
template <int IMM = 0> __device__ __forceinline__ v4u ld_async(v4u desc, int voff, int soff) {
  v4u r;
  asm volatile("buffer_load_dwordx4 %0, %1, %2, %3 offen offset:%c4 sc1" : "=v"(r) : "v"(voff), "s"(desc), "s"(soff), "i"(IMM) : "memory");
  return r;
}
template <int IMM = 0> __device__ __forceinline__ v3u ld_async3(v4u desc, int voff, int soff) {
  v3u r;
  asm volatile("buffer_load_dwordx3 %0, %1, %2, %3 offen offset:%c4 sc1" : "=v"(r) : "v"(voff), "s"(desc), "s"(soff), "i"(IMM) : "memory");
  return r;
}
__device__ __forceinline__ unsigned ld_async1(v4u desc, int voff, int soff) {
  unsigned r;
  asm volatile("buffer_load_dword %0, %1, %2, %3 offen sc1" : "=v"(r) : "v"(voff), "s"(desc), "s"(soff) : "memory");
  return r;
}
template <int N, class A> __device__ __forceinline__ void landed(A& a) { asm volatile("s_waitcnt vmcnt(%c1)" : "+v"(a) : "i"(N) : "memory"); }
template <int N, class A, class B> __device__ __forceinline__ void landed(A& a, B& b) { asm volatile("s_waitcnt vmcnt(%c2)" : "+v"(a), "+v"(b) : "i"(N) : "memory"); }
__device__ __forceinline__ void set_m0_nonop(int lds) { asm volatile("s_mov_b32 m0, %0" :: "s"(lds) : "memory"); }
template <class T> __device__ __forceinline__ void pin(T& x) { asm volatile("" : "+v"(x)); }
__device__ __forceinline__ unsigned lds_count() {
  unsigned old;
  asm volatile("s_mov_b64 exec, 1\n\tds_add_rtn_u32 %0, %1, %2\n\ts_mov_b64 exec, -1" : "=v"(old) : "v"(LDS_CNT), "v"(1) : "memory");
  return old;
}
__device__ __forceinline__ void wait_lds0(unsigned& x) { asm volatile("s_waitcnt lgkmcnt(0)\n\ts_nop 0" : "+v"(x) :: "memory"); }
__device__ __forceinline__ void ld_epoch(v4u wsd) {
  asm volatile("s_mov_b32 m0, %0\n\ts_mov_b64 exec, 1\n\tbuffer_load_dword off, %1, 0 sc1 lds\n\ts_mov_b64 exec, -1" :: "s"(LDS_EP), "s"(wsd) : "memory");
}
__device__ __forceinline__ void flag_store(v4u wsd, int soff, unsigned vtag) {
  asm volatile("s_mov_b64 exec, 1\n\tbuffer_store_dword %0, off, %1, %2 sc1\n\ts_mov_b64 exec, -1" :: "v"(vtag), "s"(wsd), "s"(soff) : "memory");
}
constexpr int OOB = 0x40000000;
template <class T> __device__ __forceinline__ void pins(T x) { asm volatile("" :: "s"(x)); }
#define BAR() do { asm volatile("" ::: "memory"); __builtin_amdgcn_s_waitcnt(0xC07F); asm volatile("s_barrier" ::: "memory"); } while (0)
#define WAITV(n) asm volatile("s_waitcnt vmcnt(%c0)" :: "i"(n) : "memory")
#define FENCE() asm volatile("" ::: "memory")
#define SB() __builtin_amdgcn_sched_barrier(0)
#define PIN(x) pin(x)
#define OPQ(x) asm("" : "+v"(x))
#define SC1 16

#define STAMPV(k)

template <int CTRL, int RM>
__device__ __forceinline__ float dpp_add(float v) {
  return v + __builtin_bit_cast(float, __builtin_amdgcn_update_dpp(0, __builtin_bit_cast(int, v), CTRL, RM, 0xf, true));
}
__device__ __forceinline__ void wave_sum2(float& a, float& b) {
  auto r = __builtin_amdgcn_permlane32_swap(__float_as_uint(a), __float_as_uint(b), false, false);
  float t = __uint_as_float(r[0]) + __uint_as_float(r[1]);
  t = dpp_add<0xB1, 0xf>(t);
  t = dpp_add<0x4E, 0xf>(t);
  t = dpp_add<0x141, 0xf>(t);
  t = dpp_add<0x140, 0xf>(t);
  t = dpp_add<0x142, 0xa>(t);
  a = __builtin_bit_cast(float, __builtin_amdgcn_readlane(__builtin_bit_cast(int, t), 31));
  b = __builtin_bit_cast(float, __builtin_amdgcn_readlane(__builtin_bit_cast(int, t), 63));
}

__device__ __forceinline__ void half_sum2(float& a, float& b) {
  auto r = __builtin_amdgcn_permlane16_swap(__float_as_uint(a), __float_as_uint(b), false, false);
  float t = __uint_as_float(r[0]) + __uint_as_float(r[1]);
  t = dpp_add<0xB1, 0xf>(t);
  t = dpp_add<0x4E, 0xf>(t);
  t = dpp_add<0x141, 0xf>(t);
  t = dpp_add<0x140, 0xf>(t);
  r = __builtin_amdgcn_permlane16_swap(__float_as_uint(t), __float_as_uint(t), false, false);
  a = __uint_as_float(r[0]); b = __uint_as_float(r[1]);
}
__device__ __forceinline__ void opq4(f32x2 (&t)[4]) { asm("" : "+v"(t[0]), "+v"(t[1]), "+v"(t[2]), "+v"(t[3])); }

__device__ __forceinline__ f32x2 cmul(f32x2 a, f32x2 e) {
  f32x2 r = {-e.y, e.x};
  return fma2(a.yy, r, a.xx * e);
}
__device__ __forceinline__ f32x2 cexp_l(float a2, float bt, float lam) {
  float m = __builtin_amdgcn_exp2f(a2 * lam);
  float ph = __builtin_amdgcn_fractf(bt * lam);
  f32x2 r = {__builtin_amdgcn_cosf(ph), __builtin_amdgcn_sinf(ph)};
  return r * m;
}
__device__ __forceinline__ f32x2 xsig2(f32x2 x, f32x2 t) {
  OPQ(t);
  f32x2 e = {__builtin_amdgcn_exp2f(t.x), __builtin_amdgcn_exp2f(t.y)};
  e = e + 1.f;
  OPQ(e);
  f32x2 r = {__builtin_amdgcn_rcpf(e.x), __builtin_amdgcn_rcpf(e.y)};
  return x * r;
}

__device__ __forceinline__ bool all4(v4u f, unsigned tag) { return __all(f.x == tag && f.y == tag && f.z == tag && f.w == tag); }
__device__ __forceinline__ void poll4(rsrc_t wsr, int voff, unsigned tag) {
#pragma unroll 1
  for (int spin = 0; spin < (1 << 18); ++spin) {
    v4u f = __builtin_amdgcn_raw_buffer_load_b128(wsr, voff, 0, SC1);
    if (all4(f, tag)) break;
    __builtin_amdgcn_s_sleep(2);
  }
  FENCE();
}
__device__ __forceinline__ void poll1(rsrc_t wsr, int voff, unsigned tag) {
#pragma unroll 1
  for (int spin = 0; spin < (1 << 18); ++spin) {
    unsigned f = __builtin_amdgcn_raw_buffer_load_b32(wsr, voff, 0, SC1);
    if (__all(f == tag)) break;
    __builtin_amdgcn_s_sleep(2);
  }
  FENCE();
}

struct Ctx {
  rsrc_t wsr;
  int lane16;
  int w2off;
  int w2step;
  int w2last;
  int tb, ub;
  int pw, pr;
  int uf;
  int so;
  unsigned vtag;
  v4u wsd;
};

template <int I>
__device__ __forceinline__ void ldw2(const Ctx& c, v4u (&W2)[35]) {
  constexpr int s = I / 5, j = I % 5;
  W2[I] = ld_async<(j & 3) * 1024>(c.wsd, c.lane16, (s < 6 ? c.w2off + s * c.w2step : c.w2last) + (j >> 2) * 4096);
}
template <int LO, int HI>
__device__ __forceinline__ void ldw2r(const Ctx& c, v4u (&W2)[35]) {
  if constexpr (LO < HI) { ldw2<LO>(c, W2); ldw2r<LO + 1, HI>(c, W2); }
}

#define SIGM 17
struct ConvPlan {
  int p[36], q[36];
  int unit[16];
  int at[16];
  int done[8];
};
constexpr ConvPlan conv_plan() {
  ConvPlan c{};
  constexpr int pq[36][2] = {{0,0},{1,0},{2,0},{0,1},{1,1},{3,0},{0,2},{2,1},{4,0},{1,2},{3,1},{0,3},{2,2},{5,0},{1,3},{4,1},{0,4},{3,2},
                             {6,0},{2,3},{5,1},{1,4},{4,2},{0,5},{3,3},{7,0},{2,4},{6,1},{1,5},{5,2},{0,6},{4,3},{3,4},{2,5},{1,6},{0,7}};
  int n = 0; bool seen[16] = {};
  for (int m = 0; m < 36; ++m) {
    c.p[m] = pq[m][0]; c.q[m] = pq[m][1]; c.done[pq[m][0] + pq[m][1]] = m;
    for (int k = 0; k < 2; ++k) { int u = k * 8 + pq[m][k]; if (!seen[u]) { seen[u] = true; c.unit[n] = u; c.at[n] = m; ++n; } }
  }
  return c;
}
template <int LO, int HI, bool SIG, class MID, class TAIL, class POST>
__device__ __forceinline__ void conv_round(const Ctx& c, int tb, int ub, int uf, int chpair, int dk_off, int fh_off, v4u (&W2)[35], MID&& mid, TAIL&& tail, POST&& post
) {
  constexpr ConvPlan pl = conv_plan();
  constexpr int NL = HI - LO, PRE = 6;
  f32x4 acc[8];
  v4u F[2][16];
  unsigned old;
  auto rd = [&](auto I) {
    constexpr int rho = I.v >> 4, u = pl.unit[I.v & 15], off = rho ? 0 : 64;
    if constexpr (u >= 8) F[rho][u] = LD(v4u, ub + off + (u - 8) * 512);
    else {
      v2u t0 = LDV(v2u, tb + off + (7 - u) * 512), t1 = LDV(v2u, tb + off + (7 - u) * 512 + 8);
      F[rho][u] = (v4u){t0.x, t0.y, t1.x, t1.y};
    }
  };
  rd(ic<0>{}); rd(ic<1>{}); rd(ic<2>{}); rd(ic<3>{}); rd(ic<4>{}); rd(ic<5>{});
  SB();
  auto step = [&](auto M) {
    constexpr int m = M.v, rho = m / 36, k = m % 36, p = pl.p[k], q = pl.q[k];
    if constexpr (rho == 0 && q == 0) mfma_z(acc[p + q], F[rho][p], F[rho][8 + q]); else mfma_ip(acc[p + q], F[rho][p], F[rho][8 + q]);
    SB();
    constexpr int u = m < 4 ? PRE + m : (m & 1) ? -1 : m < 16 ? PRE + 2 + m / 2 : m < 24 ? -1 : m < 56 ? 16 + (m - 24) / 2 : -1;
    if constexpr (u >= 0) rd(ic<u>{});
    if constexpr (m % 6 == 5) ldw2r<LO + ((m / 6) * NL + 11) / 12, LO + ((m / 6 + 1) * NL + 11) / 12>(c, W2);
    if constexpr (SIG && m == SIGM) {
      WAITV((((SIGM + 1) / 6) * NL + 11) / 12);
      old = lds_count();
    }
    if constexpr (SIG && m == SIGM + 5) {
      wait_lds0(old);
      int fo = __builtin_amdgcn_readfirstlane(old) == 7 ? fh_off : OOB;
      flag_store(c.wsd, fo, c.vtag);
    }
    mid(M);
    if constexpr (rho == 1) {
      constexpr int t = k == 9 ? 0 : k == 13 ? 1 : k == 17 ? 2 : k == 20 ? 3 : k == 25 ? 4 : k == 32 ? 5 : -1;
      if constexpr (t >= 0) LDV(f32x4, c.pw + t * 1024) = acc[t];
    }
    SB();
  };
  static_for<72>(step);
  asm volatile("s_nop 2" : "+v"(acc[6]));
  LDV(f32x4, c.pw + 6 * 1024) = acc[6];
  asm volatile("s_nop 3" : "+v"(acc[7]));
  LDV(f32x4, c.pw + 7 * 1024) = acc[7];
  tail();
  if constexpr (SIG) STAMPV(34);
  BAR();
  if constexpr (SIG) STAMPV(35);
  {
    f32x4 p[8];
#pragma unroll
    for (int i = 0; i < 8; ++i) p[i] = LD(f32x4, c.pr + i * 8192);
    v2u u0 = LD(v2u, uf), u1 = LD(v2u, uf + U_STR);
    unsigned dkp = LD(unsigned, dk_off);
    post();
    f32x4 y0 = (p[0] + p[1]) + (p[2] + p[3]), y1 = (p[4] + p[5]) + (p[6] + p[7]);
    float dk0 = bflo(dkp), dk1 = bfhi(dkp);
    f32x2 a = fma2(unpk(u0.x), (f32x2){dk0, dk0}, y0.xy), b = fma2(unpk(u0.y), (f32x2){dk0, dk0}, y0.zw);
    f32x2 d = fma2(unpk(u1.x), (f32x2){dk1, dk1}, y1.xy), e = fma2(unpk(u1.y), (f32x2){dk1, dk1}, y1.zw);
    v4u o = {pk2(a.x, d.x), pk2(a.y, d.y), pk2(b.x, e.x), pk2(b.y, e.y)};
    __builtin_amdgcn_raw_buffer_store_b128(o, c.wsr, c.so + chpair * 8192, 0, SC1);
  }
  FENCE();
}
__device__ __forceinline__ void conv_addr_a(Ctx& cx, int lane, int wave) {
  const int l15 = lane & 15, g16 = lane & 48;
  const int ch = wave >> 2, rho = (wave & 3) * 2 + 1;
  cx.tb = LDS_KCL + ch * 4 * KC_STR + (2028 - 256 * 7 - 32 * rho) * 2 + (l15 & 3) * KC_STR + g16 - 2 * (l15 & 12);
  cx.ub = LDS_U + ch * U_STR + (256 - 32 * rho - 16) * 2 + 32 * l15 + g16;
  PIN(cx.tb); PIN(cx.ub);
}
__device__ __forceinline__ void conv_addr_b(Ctx& cx, int tid, int wave) {
  const int lane = tid & 63, l15 = lane & 15, g = lane >> 4;
  cx.pw = LDS_CP + wave * 8192 + g * 256 + ((l15 ^ (4 * g)) << 4);
  cx.pr = LDS_CP + wave * 1024 + (lane & 3) * 256 + (((lane >> 2) ^ (4 * (lane & 3))) << 4);
  cx.uf = LDS_U + 512 + tid * 8;
  cx.so = WS_ST + tid * 16;
  PIN(cx.pw); PIN(cx.pr); PIN(cx.uf); PIN(cx.so);
}
#define SV

constexpr int KD_DUMMY = LDS_KD + KD_STR - 8;
struct KAddr { int fa, fb, kd[4]; };
__device__ __forceinline__ void kaddr_frag(KAddr& k, int lane, int wave) {
  const int l15 = lane & 15, g16 = lane & 48, base = LDS_KS + (wave >> 2) * KCH;
  int ur = (wave & 2) * 8 + l15; ur = ur < 21 ? ur : 20;
  k.fa = base + ur * KROW + g16;
  k.fb = base + (21 + (wave & 1) * 16) * KROW + l15 * KROW + g16;
  PIN(k.fa); PIN(k.fb);
}
__device__ __forceinline__ void kaddr_scatter(KAddr& k, int lane, int wave) {
  const int l15 = lane & 15, g = lane >> 4, kd = LDS_KD + (wave >> 2) * KD_STR + 32;
  const int j = (wave & 1) * 16 + l15;
  const bool lo = (wave & 2) == 0;
  const unsigned sel = 0x0c0c0c00u | g;
  const int sh = __builtin_amdgcn_perm(0u, lo ? 0x02030404u : 0x01010101u, sel), b8 = __builtin_amdgcn_perm(0u, lo ? 0x60400100u : 0xa0a0a080u, sel);
  const int d2 = __builtin_amdgcn_perm(0u, lo ? 0x80040404u : 0x80808080u, sel);
  const int a0 = kd + (j << sh) + b8 * 8, d1 = lo ? 2 : 64;
  const bool on = lo || g == 0;
  k.kd[0] = (on || (g == 1 && j == 0)) ? a0 : KD_DUMMY;
  k.kd[1] = on ? a0 + d1 : KD_DUMMY;
  k.kd[2] = on ? a0 + d2 : KD_DUMMY;
  k.kd[3] = on ? a0 + d1 + d2 : KD_DUMMY;
  PIN(k.kd[0]); PIN(k.kd[1]); PIN(k.kd[2]); PIN(k.kd[3]);
}
template <int OFF>
__device__ __forceinline__ void kscatter(const KAddr& k, f32x4 d) {
  unsigned p01 = pk2(d.x, d.y), p23 = pk2(d.z, d.w);
  LD(u16, k.kd[0] + OFF) = (u16)p01; LD(u16, k.kd[1] + OFF) = (u16)(p01 >> 16); LD(u16, k.kd[2] + OFF) = (u16)p23; LD(u16, k.kd[3] + OFF) = (u16)(p23 >> 16);
}
struct EAddr { float lf; int kb, ex[11], ew; };
__device__ __forceinline__ void eaddr_init(EAddr& e, int tid, int wave) {
  const int q = tid & 255, l0 = 2047 - 8 * q, lm = l0 < 256 ? 256 : l0;
  e.lf = (float)lm;
  e.kb = LDS_KD + (wave >> 2) * KD_STR + 32 + 2 * (128 - 0x4300) + 2 * (l0 - lm);
  e.ew = LDS_KCL + (wave >> 2) * 4 * KC_STR + q * 16;
  PIN(e.lf); PIN(e.kb); PIN(e.ew);
}
template <int K>
__device__ __forceinline__ void eaddr_pair(EAddr& e) {
  float t;
  if constexpr (K < 5)
    asm volatile("v_add_f32 %2, -1.0, %3\n\tv_cvt_pk_bf16_f32 %2, %3, %2\n\tv_add_f32 %3, -2.0, %3\n\tv_mad_u32_u16 %0, %2, 2, %4\n\tv_mad_u32_u16 %1, %2, 2, %4 op_sel:[1,0,0,0]"
                 : "=&v"(e.ex[2 * K]), "=&v"(e.ex[2 * K + 1]), "=&v"(t), "+v"(e.lf) : "v"(e.kb));
  else
    asm volatile("v_cvt_pk_bf16_f32 %1, %2, %2\n\tv_mad_u32_u16 %0, %1, 2, %3" : "=&v"(e.ex[2 * K]), "=&v"(t) : "v"(e.lf), "v"(e.kb));
}
__device__ __forceinline__ unsigned pair16(unsigned hi, unsigned lo) { return __builtin_amdgcn_perm(hi, lo, 0x05040100u); }
__device__ __forceinline__ void kcopies(const unsigned (&ev)[11], int kc) {
  unsigned P[5], Q[5];
#pragma unroll
  for (int k = 0; k < 5; ++k) { P[k] = pair16(ev[2 * k + 1], ev[2 * k]); Q[k] = pair16(ev[2 * k + 2], ev[2 * k + 1]); }
  LD(v4u, kc + 0 * KC_STR) = (v4u){Q[1], Q[2], Q[3], Q[4]};
  LD(v4u, kc + 1 * KC_STR) = (v4u){P[1], P[2], P[3], P[4]};
  LD(v4u, kc + 2 * KC_STR) = (v4u){Q[0], Q[1], Q[2], Q[3]};
  LD(v4u, kc + 3 * KC_STR) = (v4u){P[0], P[1], P[2], P[3]};
}

#define PF0 3
#define PFPOS 2
#define CODEPF 1
constexpr int CODE_LINES = 256;
#define MAP84 0
#define WREV 0
#define HROT 0
#define FHW 7
#define FHM 46
#define GB1 0
#define GB2 3
constexpr int W2A = 17, W2B = 30;
#define ROTMODE 0

struct Desc { const void* p; unsigned bytes, w3; };
struct Args { Desc d[15]; };

__global__ __launch_bounds__(512, 2) void fused_kernel(Args args) {
  STAMPV(0);
  const int tid = threadIdx.x, lane = tid & 63;
  const unsigned long long ka = (unsigned long long)__builtin_amdgcn_kernarg_segment_ptr();
  v16u d4; v4u wsd, tabd, biasd;
  asm volatile("s_load_dwordx16 %0, %2, 0xa0\n\ts_load_dwordx4 %1, %2, 0xe0" : "=&s"(d4), "=&s"(wsd) : "s"(ka));
  const int wave = __builtin_amdgcn_readfirstlane(tid >> 6);
  const int bid = blockIdx.x;
  const bool aux = bid >= 192;
  asm volatile("s_load_dwordx4 %0, %2, %3\n\ts_load_dwordx4 %1, %2, %4"
               : "=&s"(tabd), "=&s"(biasd) : "s"(ka), "s"((int)(((aux ? 0x76543210u : 0x6420999eu) >> (wave * 4)) & 15) * 16),
                 "s"((int)(((aux ? 0x98989898u : 0x75310000u) >> (wave * 4)) & 15) * 16));
  if (wave == 3) {
    unsigned long long pc;
    asm volatile("s_getpc_b64 %0" : "=s"(pc));
    const int ln = (bid >> 3) * 8 + (lane & 7);
    asm volatile("global_load_dword v255, %0, %1" :: "v"((ln < CODE_LINES ? ln : CODE_LINES - 1) * 128), "s"(pc & ~255ull) : "memory", "v255");
  }
  const int l15 = lane & 15, g = lane >> 4, lane16 = lane * 16;
  int vo[5], swb, sxb, rot;
  unsigned pv[7];
  const int xcd = bid & 7, slot = bid >> 3;
  const int tb = xcd * 32 + slot;
  const int ch0 = xcd * 80 + slot * 2 + (slot > 24 ? slot - 24 : 0) * 2;
  const int gm = (xcd >> 1) * 4 + (slot & 3), gn = (xcd & 1) * 6 + (slot >> 2);
  const int mb = xcd * 4 + (slot & 3), nb = slot >> 2;
  const int wrev = WREV ? mb & 1 : 0, hrot = HROT ? (nb & 1) * 2 : 0;
  const int ai = bid - 192, cg = ai < 24 ? (ai & 1) * 12 + (ai >> 1) : ai;
  const int s0 = wave * 7 - (wave > 6 ? 1 : 0), w2off = WS_W2T + ((s0 + 5 * wrev) * 40 + nb * 5) * 1024;
  {
    const int c0 = ch0, s_ln = tb * 10240;
    const int m_a = wave == 0 ? LDS_EP : LDS_B2, s_a = wave == 0 ? 0 : nb * 160;
    const int s_b = wave == 7 ? nb * 160 : cg * 128;
    pins(m_a); pins(s_a); pins(s_b); pins(w2off); pins(gm); pins(gn); pins(mb); pins(s0);
    vo[2] = (lane >> 3) * 1280 + (((lane & 7) ^ g) << 4);
    vo[0] = g * 3456 + ((l15 ^ ((g << 2) | ((wave * 2) & 3))) << 4);
    vo[1] = (vo[0] ^ 16) + 4 * 3456 - 1024;
    vo[3] = (vo[2] ^ 64) + 8 * 1280 - 1024;
    rot = 0;
    swb = wave * 8 * 3456 + gn * 256;
    sxb = (gm * 128 + wave * 16) * 1280;
    PIN(vo[0]); PIN(vo[1]); PIN(vo[2]); PIN(vo[3]);
    pins(c0); pins(s_ln); pins(swb + rot * (64 * 3456)); pins(sxb + rot * 128); pins(wave * 4096 + 2048);
    STAMPV(60);
    asm volatile("s_waitcnt lgkmcnt(0)" : "+s"(d4), "+s"(tabd), "+s"(wsd), "+s"(biasd));
    if (!aux && wave >= 4) {
      const int sw = swb + rot * (64 * 3456), sx = sxb + rot * 128;
      set_m0(wave * 4096 + 2048); glds_o<0>(d4.s4567, vo[0], sw); glds_o<1024>(d4.s4567, vo[1], sw);
      set_m0(wave * 4096); glds_o<0>(d4.s0123, vo[2], sx); glds_o<1024>(d4.s0123, vo[3], sx);
      set_m0(wave * 4096 + 2048 - 16384); glds_o<0>(d4.s4567, vo[0], sw - 32 * 3456); glds_o<1024>(d4.s4567, vo[1], sw - 32 * 3456);
      set_m0(wave * 4096 - 16384); glds_o<0>(d4.s0123, vo[2], sx - 64 * 1280); glds_o<1024>(d4.s0123, vo[3], sx - 64 * 1280);
    } else if (!aux) {
      set_m0(LDS_LN_MAIN + wave * 2560);
      glds_o<0>(d4.s0123, lane16, s_ln + wave * 2560); glds_o<1024>(d4.s0123, lane16, s_ln + wave * 2560);
      glds_m(d4.s0123, LDS_LN_MAIN + wave * 2560 + 2048, lane16, s_ln + wave * 2560 + 2048, 0xffffffffull);
      if (wave < 2) glds_s(tabd, m_a, lane16, s_a, wave == 1 ? 0x3ffull : 1ull);
    } else {
      const bool tab = wave < 6;
      const int m_ln = LDS_LN_AUX + wave * 1024;
      glds(d4.s0123, m_ln, lane16, s_ln + wave * 1024);
      glds_m(tabd, wave == 7 ? LDS_DK : LDS_PAR_AUX + wave * 2048, lane16, c0 << (tab ? 8 : 1), tab ? ~0ull : 1ull);
      if (!tab) {
        glds(d4.s0123, m_ln + 2048, lane16, s_ln + wave * 1024 + 2048);
        glds_m(biasd, LDS_B1 + (wave - 6) * 256, lane16, s_b, 0x3ffull);
        ld_epoch(wsd);
      }
    }
    STAMPV(61);
  }
  const v4u xd = d4.s0123, w1d = d4.s4567, w2d = d4.s89ab, od = d4.scdef;
  const int m0w = wave * 4096;
  const int lds_ln = LDS_LN_AUX, lds_par = LDS_PAR_AUX;

  const rsrc_t wsr = as_rsrc(wsd);
  const int kc_ = wave >> 2;
  const int fg = wave >> 1, th = wave & 1, hh = lane >> 5;
  int a_ln, a_ln4, a_tp, o_xn, a_par;
  auto ln_addr = [&]() {
    a_ln = lds_ln + wave * 1280 + lane * 8; a_ln4 = lds_ln + wave * 1280 + 1024 + lane * 4;
    a_tp = lds_ln + wave * 128 + (l15 >> 2) * 1280 + g * 32 + (lane & 3) * 8;
    o_xn = WS_XNB + (tb * 640 + wave * 64) * 16 + lane16;
    a_par = lds_par + kc_ * 256 + (wave & 2) * 64 + lane * 2;
    LD(unsigned, LDS_CNT) = 0u;
    PIN(a_ln); PIN(a_ln4); PIN(a_tp); PIN(o_xn); PIN(a_par);
  };
  unsigned epoch;
  float m1, den1, s2 = 0.f, q2 = 0.f;
  auto ln_row = [&](auto&& hook) {
    v2u a = LD(v2u, a_ln), b = LD(v2u, a_ln + 512);
    unsigned v[5] = {a.x, a.y, b.x, b.y, LD(unsigned, a_ln4)};
    epoch = LD(unsigned, LDS_EP);
    hook(ic<0>{});
    float s = 0.f, q = 0.f;
#pragma unroll
    for (int i = 0; i < 5; ++i) { s = dot2(v[i], 0x3f803f80u, s); q = dot2(v[i], v[i], q); }
    wave_sum2(s, q);
    hook(ic<1>{});
    float md = s * (1.f / D_);
    float var = fmaxf(q * (1.f / D_) - md * md, 0.f);
    m1 = rbf(md); den1 = rbf(__builtin_amdgcn_sqrtf(rbf(rbf(var) + 1e-5f)));
    const float r1 = __builtin_amdgcn_rcpf(den1), c1 = -m1 * r1;
#pragma unroll
    for (int i = 0; i < 5; ++i) {
      f32x2 w = fma2(unpk(v[i]), (f32x2){r1, r1}, (f32x2){c1, c1});
      v[i] = pk2(w.x, w.y);
    }
    LD(v2u, a_ln) = (v2u){v[0], v[1]};
    LD(v2u, a_ln + 512) = (v2u){v[2], v[3]};
    LD(unsigned, a_ln4) = v[4];
#pragma unroll
    for (int i = 0; i < 5; ++i) { s2 = dot2(v[i], 0x3f803f80u, s2); q2 = dot2(v[i], v[i], q2); }
  };
  auto ln_publish = [&]() {
    v2u lo = trread(a_tp), hi = trread(a_tp + 4 * 1280), lo2 = trread(a_tp + 1024), hi2 = trread(a_tp + 1024 + 4 * 1280);
    __builtin_amdgcn_raw_buffer_store_b128((v4u){lo.x, lo.y, hi.x, hi.y}, wsr, o_xn, 0, SC1);
    __builtin_amdgcn_raw_buffer_store_b128((v4u){lo2.x, lo2.y, hi2.x, hi2.y}, wsr, o_xn + (wave < 2 ? 8192 : OOB), 0, SC1);
    FENCE();
  };
  v4u rowf, rowc;
  auto ln_stats = [&](float ssum, float qsum) {
    const float mm = ssum * (1.f / D_);
    const float den2 = rbf(__builtin_amdgcn_sqrtf(rbf(rbf(fmaxf(qsum * (1.f / D_) - mm * mm, 0.f)) + 1e-5f)));
    const float mu = -m1 - rbf(mm) * den1, rho = den1 * den2, al = __builtin_amdgcn_rcpf(rho);
    const unsigned hi = pk2(mu, rho), lo = pk2(mu - bflo(hi), rho - bfhi(hi));
    rowf = (v4u){__builtin_amdgcn_perm(hi, hi, 0x01000100u), __builtin_amdgcn_perm(hi, lo, 0x07060100u), lo >> 16, 0u};
    const float k1 = -2.302208198f * al;
    rowc = (v4u){__float_as_uint(k1 * al * al * 0.044715f), __float_as_uint(k1), __float_as_uint(rho), 0u};
    PIN(rowf); PIN(rowc);
  };
  auto ln_stats_store = [&](int off) {
    __builtin_amdgcn_raw_buffer_store_b128(rowf, wsr, off, 0, SC1);
    __builtin_amdgcn_raw_buffer_store_b128(rowc, wsr, off + 16, 0, SC1);
    FENCE();
  };

  unsigned tag;
  Ctx cx;
  cx.wsr = wsr; cx.wsd = wsd; cx.lane16 = lane16;
  cx.w2off = w2off;
  cx.w2step = wrev ? -40960 : 40960;
  cx.w2last = wave < 6 ? cx.w2off + (6 - 5 * wrev) * 40960 : OOB;
  v4u W2[35];
  KAddr kad; EAddr ead;
  int a_gen, ugo, uwo, zao;
  auto misc_addr = [&]() {
    ugo = WS_XNB + ((tid >> 1) * 640 + ch0 + (tid & 1)) * 16;
    uwo = LDS_U + (tid & 1) * U_STR + 512 + (tid >> 1) * 16;
    PIN(ugo); PIN(uwo);
  };
  auto misc_addr2 = [&]() {
    a_gen = LDS_KS + kc_ * KCH + (th ? 0 : 21 * KROW) + (wave & 2) * 128 + lane * 4;
    zao = lane < 2 ? LDS_KCL + wave * KC_STR + 4096 + lane16 : LDS_U + (wave & 3) * U_STR + (lane & 31) * 16;
    PIN(a_gen); PIN(zao);
  };
  auto gelu = [&](const f32x2 (&a)[4], v3u ca, v3u cb, unsigned (&hf)[4], auto&& hook) {
    const f32x3 c[2] = {{__uint_as_float(ca.x), __uint_as_float(ca.y), __uint_as_float(ca.z)}, {__uint_as_float(cb.x), __uint_as_float(cb.y), __uint_as_float(cb.z)}};
    f32x2 t[4];
#pragma unroll
    for (int i = 0; i < 4; ++i) t[i] = a[i] * a[i];
#pragma unroll
    for (int i = 0; i < 4; ++i) t[i] = fma2(t[i], c[i >> 1].xx, c[i >> 1].yy);
#pragma unroll
    for (int i = 0; i < 4; ++i) t[i] = t[i] * a[i];
    opq4(t);
    SB(); hook(ic<0>{}); SB();
#pragma unroll
    for (int i = 0; i < 4; ++i) { t[i] = (f32x2){__builtin_amdgcn_exp2f(t[i].x), __builtin_amdgcn_exp2f(t[i].y)}; if (i == 1) { SB(); hook(ic<1>{}); SB(); } }
    SB();
#pragma unroll
    for (int i = 0; i < 4; ++i) t[i] = fma2(t[i], c[i >> 1].zz, c[i >> 1].zz);
    opq4(t);
    SB(); hook(ic<2>{}); SB();
#pragma unroll
    for (int i = 0; i < 4; ++i) { t[i] = (f32x2){__builtin_amdgcn_rcpf(t[i].x), __builtin_amdgcn_rcpf(t[i].y)}; if (i == 1) { SB(); hook(ic<3>{}); SB(); } }
    SB(); hook(ic<4>{}); SB();
#pragma unroll
    for (int i = 0; i < 4; ++i) t[i] = t[i] * a[i];
#pragma unroll
    for (int i = 0; i < 4; ++i) hf[i] = pk2(t[i].x, t[i].y);
  };
  float a2, bt, dt, e_m, e_p;
  cf E[2];
  auto gsetup = [&](auto N, float lam0, float lam1, float sg) {
    constexpr int n = N.v;
    if constexpr (n == 0) { dt = rbf(__builtin_amdgcn_exp2f(bflo(pv[6]) * 1.44269504088896f)); PIN(dt); }
    if constexpr (n == 1) { a2 = vmul(dt, bflo(pv[0])) * 1.44269504088896f; bt = vmul(dt, bflo(pv[1])) * (sg * 0.15915494309189535f); PIN(a2); PIN(bt); }
    if constexpr (n == 2 || n == 4) { e_m = __builtin_amdgcn_exp2f(a2 * (n == 2 ? lam0 : lam1)); e_p = __builtin_amdgcn_fractf(bt * (n == 2 ? lam0 : lam1)); PIN(e_m); PIN(e_p); }
    if constexpr (n == 3 || n == 5) { float c = __builtin_amdgcn_cosf(e_p), s_ = __builtin_amdgcn_sinf(e_p); PIN(c); PIN(s_); E[n == 5].x = c * e_m; PIN(E[n == 5].x); E[n == 5].y = s_ * e_m; PIN(E[n == 5].y); }
  };

  if (!aux && wave < 4) {
    STAMPV(1);
    const int fh = wave >> 1, l31 = lane & 31;
    int a_r = LDS_LN_MAIN + wave * 2560 + hh * 1280 + l31 * 16, a_r8 = a_r + 1024 - l31 * 8;
    int xb[2][4], wb[2][4];
    xb[0][0] = th * 16384 + (g & 1) * 4096 + l15 * 128 + ((hh ^ (l15 >> 1)) << 4);
    wb[0][0] = hh * 4096 + 2048 + (l15 >> 2) * 256 + (((8 * fh + 2 * (g & 1) + ((lane >> 1) & 1)) ^ ((l15 & 12) | (2 * hh))) << 4) + 8 * (lane & 1);
#pragma unroll
    for (int i = 0; i < 4; ++i) {
      xb[0][i] = xb[0][0] ^ (i * 32); xb[1][i] = xb[0][i] + 65536; wb[0][i] = wb[0][0] ^ ((i >> 1) * 64 + (i & 1) * (1024 + 16)); wb[1][i] = wb[0][i] + 65536;
      PIN(xb[0][i]); PIN(xb[1][i]); PIN(wb[0][i]); PIN(wb[1][i]);
    }
    PIN(a_r); PIN(a_r8);
    conv_addr_a(cx, lane, wave); conv_addr_b(cx, tid, wave);
    int hoff = WS_HT + ((gm * 8 + th * 4) * KS2 + gn * 4 + fh * 2) * 1024 + l15 * 16 + hh * 256 + (g & 1) * (KS2 * 1024);
    int o_s = LDS_RS + wave * 64 + hh * 32, o_c = hh ? OOB : lane16;
    a_par = LDS_PAR_MAIN + wave * 128 + lane * 2;
    a_gen = LDS_KS + fh * KCH + th * 256 + lane * 4;
    PIN(hoff); PIN(o_s); PIN(o_c); PIN(a_gen); PIN(a_par);
    LD(unsigned, LDS_CNT) = 0u;
    SB();
    STAMPV(2);
    WAITV(0);
    STAMPV(3);
    {
      v4u a = LD(v4u, a_r), b = LD(v4u, a_r + 512);
      v2u c = LD(v2u, a_r8);
      unsigned v[10] = {a.x, a.y, a.z, a.w, b.x, b.y, b.z, b.w, c.x, c.y};
      float s = 0.f, q = 0.f;
#pragma unroll
      for (int i = 0; i < 10; ++i) { s = dot2(v[i], 0x3f803f80u, s); q = dot2(v[i], v[i], q); }
      half_sum2(s, q);
      float md = s * (1.f / D_);
      float var = fmaxf(q * (1.f / D_) - md * md, 0.f);
      m1 = rbf(md); den1 = rbf(__builtin_amdgcn_sqrtf(rbf(rbf(var) + 1e-5f)));
      const float r1 = __builtin_amdgcn_rcpf(den1), c1 = -m1 * r1;
#pragma unroll
      for (int i = 0; i < 10; ++i) {
        f32x2 w = fma2(unpk(v[i]), (f32x2){r1, r1}, (f32x2){c1, c1});
        v[i] = pk2(w.x, w.y);
      }
      LD(v4u, a_r) = (v4u){v[0], v[1], v[2], v[3]};
      LD(v4u, a_r + 512) = (v4u){v[4], v[5], v[6], v[7]};
      LD(v2u, a_r8) = (v2u){v[8], v[9]};
#pragma unroll
      for (int i = 0; i < 10; ++i) { s2 = dot2(v[i], 0x3f803f80u, s2); q2 = dot2(v[i], v[i], q2); }
      half_sum2(s2, q2);
      ln_stats(s2, q2);
      if (l31 == 0) { LD(v4u, o_s) = rowf; LD(v4u, o_s + 16) = rowc; }
    }
    STAMPV(4);
    f32x16 acc1[2][2];
    v4u XA[2][4][2], WA[2][4][2];
    auto rdu = [&](auto I, auto U) {
      constexpr int i = I.v, kk = U.v >> 2, r = U.v & 3, hi = (i % 3) == 2, so = hi ? 0 : (i % 3) * 32768;
      if constexpr (r == 0 || r == 3) {
        constexpr int ft = r == 3;
        v2u lo = trread(wb[hi][2 * ft] + so + kk * 8192), up = trread(wb[hi][2 * ft + 1] + so + kk * 8192);
        WA[i & 1][kk][ft] = (v4u){lo.x, lo.y, up.x, up.y};
      } else {
        XA[i & 1][kk][r - 1] = LD(v4u, xb[hi][kk] + so + (r - 1) * 8192);
      }
    };
    auto chunk = [&](auto I, auto&& fill) {
      constexpr int i = I.v, bf = i & 1;
      static_for<16>([&](auto K) {
        constexpr int k = K.v, kk = k >> 2, ft = (k >> 1) & 1, tt = k & 1;
        if constexpr (i == 0 && kk == 0) mfma32_z(acc1[ft][tt], WA[bf][kk][ft], XA[bf][kk][tt]);
        else mfma32_ip(acc1[ft][tt], WA[bf][kk][ft], XA[bf][kk][tt]);
        SB();
        if constexpr (i + 1 < 10 && k < 12) {
          constexpr int u = (k / 3) * 4 + (k % 3 == 2 ? 3 : k % 3);
          rdu(ic<i + 1>{}, ic<u>{});
          if constexpr (k % 3 == 1) rdu(ic<i + 1>{}, ic<u + 1>{});
        }
        fill(K);
        SB();
      });
      STAMPV(14 + i);
    };
    BAR();
    epoch = LD(unsigned, LDS_EP);
    static_for<16>([&](auto U) { rdu(ic<0>{}, U); });
    cx.vtag = epoch + 1u; PIN(cx.vtag); tag = __builtin_amdgcn_readfirstlane(cx.vtag);
    STAMPV(13);
    cf W[21], E2, E4, Bv, Cv;
    unsigned wp[21];
    auto gop = [&](auto N) {
      constexpr int n = N.v;
      if constexpr (n < 6) gsetup(N, 1.f, 256.f, -1.f);
      if constexpr (n == 6) { Bv.x = vmul(dt, bflo(pv[2])); Bv.y = vmul(dt, bflo(pv[3])); }
      if constexpr (n == 7) { Cv.x = bflo(pv[4]); Cv.y = bflo(pv[5]); PIN(Cv.x); PIN(Cv.y); W[0] = cmul4(Cv, Bv); }
      if constexpr (n == 8) { W[0].y = -W[0].y; wp[0] = pk2(W[0].x, W[0].y); PIN(W[0].y); PIN(wp[0]); E2 = cmul4(E[0], E[0]); }
      if constexpr (n == 9) E4 = cmul4(E2, E2);
      if constexpr (n >= 10 && n < 30) {
        constexpr int r = n - 9;
        constexpr int src = r == 1 ? 0 : r < 4 ? r - 2 : r < 8 ? r - 4 : r < 12 ? 2 * (r - 8) : r < 14 ? 8 + 2 * (r - 12) : r < 16 ? r - 2 : r == 16 ? 14 : r - 1;
        W[r] = cmul5(W[src], r == 1 ? E[0] : r < 4 ? E2 : r < 8 ? E4 : E[1], wp[r]);
      }
    };
    auto gfill = [&](auto I) { return [&](auto K) { if constexpr (K.v % 3 == 1) gop(ic<(decltype(I)::v - 1) * 5 + K.v / 3>{}); }; };
    BAR();
    chunk(ic<0>{}, [&](auto K) {
      if constexpr (K.v < 12 && (K.v & 1) == 0) pv[K.v / 2] = LD(u16, a_par + (K.v / 2) * 512);
      if constexpr (K.v == 12) pv[6] = LD(u16, LDS_PAR_MAIN + 6 * 512 + fh * 2);
    });
    BAR();
    chunk(ic<1>{}, gfill(ic<1>{}));
    BAR();
    chunk(ic<2>{}, gfill(ic<2>{}));
    BAR();
    chunk(ic<3>{}, gfill(ic<3>{}));
    BAR();
    chunk(ic<4>{}, gfill(ic<4>{}));
    BAR();
    chunk(ic<5>{}, gfill(ic<5>{}));
    BAR();
    chunk(ic<6>{}, gfill(ic<6>{}));
    BAR();
    v4u cf_[2], rf_[2];
    v3u rc_[2];
    chunk(ic<7>{}, [&](auto K) {
      constexpr int k = K.v;
      if constexpr (k == 0 || k == 2) cf_[k / 2] = ld_async<(k / 2) * 512>(wsd, o_c, WS_CS + (gn * 128 + fh * 64) * 16);
      if constexpr (k == 5 || k == 8) rf_[k / 8] = ld_async<(k / 8) * 1024>(wsd, l31 * 32, WS_STATS + (gm * 128 + th * 64) * 32);
      if constexpr (k == 11 || k == 13) rc_[k / 13] = ld_async3<16 + (k / 13) * 1024>(wsd, l31 * 32, WS_STATS + (gm * 128 + th * 64) * 32);
    });
    BAR();
    chunk(ic<8>{}, [&](auto K) {
      if constexpr (K.v >= 11) LDV(unsigned, a_gen + (K.v - 11) * KROW) = wp[K.v - 11];
      if constexpr (K.v % 5 == 3) ldw2r<K.v / 5, K.v / 5 + 1>(cx, W2);
    });
    chunk(ic<9>{}, [&](auto K) {
      LDV(unsigned, a_gen + (5 + K.v) * KROW) = wp[5 + K.v];
      if constexpr (K.v % 5 == 2) ldw2r<3 + K.v / 5, 4 + K.v / 5>(cx, W2);
    });
    landed<6>(cf_[0], cf_[1]); PIN(rf_[0]); PIN(rf_[1]); PIN(rc_[0]); PIN(rc_[1]);
    mfma32_ip(acc1[0][0], cf_[0], rf_[0]); mfma32_ip(acc1[0][1], cf_[0], rf_[1]); mfma32_ip(acc1[1][0], cf_[1], rf_[0]); mfma32_ip(acc1[1][1], cf_[1], rf_[1]);
    asm volatile("s_nop 8" : "+v"(acc1[0][0]), "+v"(acc1[0][1]), "+v"(acc1[1][0]), "+v"(acc1[1][1]));
    STAMPV(25);
    static_for<8>([&](auto N) {
      constexpr int n = N.v, ft = n >> 2, tt = (n >> 1) & 1, pc = n & 1;
      const f32x16& A = acc1[ft][tt];
      const f32x2 ac[4] = {{A[8 * pc], A[8 * pc + 1]}, {A[8 * pc + 2], A[8 * pc + 3]}, {A[8 * pc + 4], A[8 * pc + 5]}, {A[8 * pc + 6], A[8 * pc + 7]}};
      unsigned hf[4];
      gelu(ac, rc_[tt], rc_[tt], hf, [&](auto H) {
        constexpr int h = H.v;
        if constexpr (h == 0 && 6 + 2 * n < W2A) ldw2r<6 + 2 * n, 7 + 2 * n>(cx, W2);
        if constexpr (h == 2 && 7 + 2 * n < W2A) ldw2r<7 + 2 * n, 8 + 2 * n>(cx, W2);
      });
      __builtin_amdgcn_raw_buffer_store_b128((v4u){hf[0], hf[1], hf[2], hf[3]}, wsr, hoff + tt * (2 * KS2 * 1024) + ft * 1024 + pc * 512, 0, SC1);
      SB();
      if constexpr (n == GB1) { BAR(); STAMPV(26); }
      if constexpr (n == GB2) { BAR(); STAMPV(27); }
    });
    if (bid == 0) flag_store(wsd, wave == 1 ? WS_EPOCH : OOB, cx.vtag);
    STAMPV(28);
    BAR();
    asm volatile("; end of role 0" ::: "memory");
  } else if (!aux) {
    const int bw = wave - 4, bc = bw >> 1;
#define BPRIO 1
    auto aim = [&](auto I, auto Q) {
      constexpr int i = I.v, q = Q.v;
      if constexpr (q < 8) add_m0<(i % 3) * 32768 + ((q & 2) ? 0 : 2048) - (q >> 2) * 16384>(m0w);
      else add_m0<LDS_ST + (9 - i) * 2048>(-2048 * (i < 8 ? bw : bw & 1));
    };
    auto issue = [&](auto I, auto Q) {
      constexpr int i = I.v, q = Q.v, o = q >> 2;
      const int kc = i + rot >= 10 ? i + rot - 10 : i + rot, sw = swb + kc * (64 * 3456) - o * (32 * 3456), sx = sxb + kc * 128 - o * (64 * 1280);
      if constexpr (q < 8) {
        if constexpr ((q & 3) == 0) glds_o<0>(w1d, vo[0], sw);
        if constexpr ((q & 3) == 1) glds_o<1024>(w1d, vo[1], sw);
        if constexpr ((q & 3) == 2) glds_o<0>(xd, vo[2], sx);
        if constexpr ((q & 3) == 3) glds_o<1024>(xd, vo[3], sx);
      } else {
        const int is = i + (i < 8 ? bw : bw & 1), ks = is + rot >= 10 ? is + rot - 10 : is + rot, ss = ks * (64 * 3456) + 3072 + gn * 32;
        if constexpr (q == 8) glds_o<0>(w1d, vo[4], ss);
        else glds_o<1024>(w1d, vo[4], ss + 32 * 3456 - 1024);
      }
    };
    auto issue_now = [&](auto I, auto Q) { if constexpr ((Q.v & 1) == 0) { aim(I, Q); asm volatile("s_nop 0"); } issue(I, Q); };
    STAMPV(1);
    vo[4] = ((lane >> 1) ^ ((g & 1) << 2)) * 3456 + (lane & 1) * 16;
    int xs[2][2], wsb[2];
    xs[0][0] = bw * 8192 + l15 * 128 + ((g ^ (l15 >> 1)) << 4);
    xs[0][1] = xs[0][0] ^ 64; xs[1][0] = xs[0][0] + 65536; xs[1][1] = xs[0][1] + 65536;
    wsb[0] = LDS_ST + g * 256 + (g & 1) * 128 + l15 * 8;
    wsb[1] = wsb[0] ^ 128;
    a_gen = LDS_KS + bc * KCH + 21 * KROW + (bw & 1) * 256 + lane * 4;
    a_par = LDS_PAR_MAIN + bw * 128 + lane * 2;
    int a_t = LDS_LN_MAIN + bw * 128 + (l15 >> 2) * 1280 + g * 32 + (lane & 3) * 8;
    int o_t = WS_XNB + (tb * 640 + bw * 64) * 16 + lane16;
    PIN(a_par); PIN(a_t); PIN(o_t);
    int kfa0 = LDS_KS + bc * KCH + l15 * KROW + (lane & 48), kfa1 = kfa0 + ((l15 < 4 ? l15 : 4) + 16 - l15) * KROW, kfb = kfa0 + (21 + (bw & 1) * 16) * KROW;
    int kd0, kd1, kd2, kd3;
    {
      const int j = (bw & 1) * 16 + l15, kd = LDS_KD + bc * KD_STR + 32;
      const unsigned sel = 0x0c0c0c00u | g;
      kd0 = kd + (j << __builtin_amdgcn_perm(0u, 0x02030404u, sel)) + __builtin_amdgcn_perm(0u, 0x60400100u, sel) * 8;
      kd1 = kd0 + (g < 3 ? 4 : 128);
      kd3 = g == 0 ? kd + 1024 + 2 * j : LDS_KD + 4 * KD_STR;
      kd2 = (g == 1 && j == 0) ? kd + 1280 : kd3;
    }
    PIN(vo[4]); PIN(xs[0][0]); PIN(xs[0][1]); PIN(xs[1][0]); PIN(xs[1][1]); PIN(wsb[0]); PIN(wsb[1]); PIN(a_gen);
    PIN(kfa0); PIN(kfa1); PIN(kfb); PIN(kd0); PIN(kd1); PIN(kd2); PIN(kd3);
    eaddr_init(ead, tid, 0);
    eaddr_pair<0>(ead); eaddr_pair<1>(ead); eaddr_pair<2>(ead); eaddr_pair<3>(ead); eaddr_pair<4>(ead); eaddr_pair<5>(ead);
    SB();
    STAMPV(2);
    glds_m(tabd, LDS_PAR_MAIN + bw * 1024, lane16, ch0 << (bw < 3 ? 8 : 1), bw < 3 ? 0xffffffffull : 1ull);
    glds_m(biasd, bw < 3 ? LDS_PAR_MAIN + bw * 1024 + 512 : LDS_DK, lane16, ch0 << (bw < 3 ? 8 : 1), bw < 3 ? 0xffffffffull : 1ull);
    static_for<8>([&](auto Q) { issue_now(ic<1>{}, Q); });
    static_for<8>([&](auto Q) { issue_now(ic<2>{}, Q); });
    STAMPV(4);
    f32x4 accs[2];
    v4u XS[2][2][2], WS[2];
    auto rdx = [&](auto I, auto U) {
      constexpr int i = I.v, hi = (i % 3) == 2, so = hi ? 0 : (i % 3) * 32768;
      XS[i & 1][U.v >> 1][U.v & 1] = LD(v4u, xs[hi][U.v & 1] + so + (U.v >> 1) * 4096);
    };
    auto rdw = [&](auto I, auto K) {
      v2u lo = trread(wsb[0] + (9 - I.v) * 2048 + K.v * 1024), up = trread(wsb[1] + (9 - I.v) * 2048 + K.v * 1024);
      WS[K.v] = (v4u){lo.x, lo.y, up.x, up.y};
    };
    auto smm = [&](auto I, auto K) {
      constexpr int i = I.v, k = K.v;
      if constexpr (i == 0 && k == 0) { mfma_z(accs[0], WS[0], XS[0][0][0]); mfma_z(accs[1], WS[0], XS[0][1][0]); }
      else { mfma_ip(accs[0], WS[k], XS[i & 1][0][k]); mfma_ip(accs[1], WS[k], XS[i & 1][1][k]); }
    };
    auto chunk = [&](auto I, auto&& fill) {
      constexpr int i = I.v;
      static_for<10>([&](auto K) {
        constexpr int k = K.v;
        if constexpr (i > 0 && k < 2) rdw(ic<i - 1>{}, K);
        if constexpr (i > 0 && (k == 3 || k == 4)) smm(ic<i - 1>{}, ic<k - 3>{});
        if constexpr (i + 1 < 10 && (k == 5 || k == 6)) { rdx(ic<i + 1>{}, ic<k - 5>{}); rdx(ic<i + 1>{}, ic<k - 3>{}); }
        if constexpr (i == 9 && (k == 5 || k == 6)) rdw(I, ic<k - 5>{});
        if constexpr (i == 9 && k >= 8) smm(I, ic<k - 8>{});
        if constexpr (i + 3 < 10 && k < 8) issue(ic<i + 3>{}, K);
        if constexpr ((i == 2 || i == 5) && k >= 8) issue(ic<(i == 2 ? 4 : 8)>{}, K);
        if constexpr (i + 3 < 10 && (k == 1 || k == 3 || k == 5)) aim(ic<i + 3>{}, ic<k + 1>{});
        if constexpr ((i == 2 || i == 5) && k == 7) { aim(ic<(i == 2 ? 4 : 8)>{}, ic<8>{}); asm volatile("s_nop 0"); }
        if constexpr (i + 4 < 10 && k == 9) aim(ic<i + 4>{}, ic<0>{});
        fill(K);
        SB();
      });
      STAMPV(14 + i);
    };
    WAITV(16);
    BAR();
    epoch = LD(unsigned, LDS_EP);
    static_for<4>([&](auto U) { rdx(ic<0>{}, U); });
    pv[0] = LD(u16, a_par); pv[1] = LD(u16, a_par + 512); pv[6] = LD(u16, LDS_PAR_MAIN + 6 * 512 + bc * 2);
    v4u pub[3];
#pragma unroll
    for (int i = 0; i < 3; ++i) { v2u lo = trread(a_t + i * 512), hi = trread(a_t + i * 512 + 4 * 1280); pub[i] = (v4u){lo.x, lo.y, hi.x, hi.y}; }
    int o_t2 = o_t + 8192;
    if (bw >= 2) { pub[2] = LD(v4u, LDS_RS + l15 * 16); o_t2 = (bw == 2 && lane < 16) ? WS_STATS + tb * 256 + lane16 : OOB; }
    issue_now(ic<0>{}, ic<8>{}); issue_now(ic<0>{}, ic<9>{});
    aim(ic<3>{}, ic<0>{});
    cx.vtag = epoch + 1u; PIN(cx.vtag); tag = __builtin_amdgcn_readfirstlane(cx.vtag);
    STAMPV(13);
    cf V[8], G;
    unsigned vp[32];
    auto gop = [&](auto N) {
      constexpr int n = N.v;
      if constexpr (n < 6) gsetup(N, 8.f, 64.f, 1.f);
      if constexpr (n == 6) { V[1] = E[0]; G = E[1]; vp[0] = 0x00003f80u; vp[1] = pk2(E[0].x, E[0].y); vp[8] = pk2(G.x, G.y); PIN(vp[1]); PIN(vp[8]); }
      if constexpr (n == 7) V[2] = cmul5(V[1], V[1], vp[2]);
      if constexpr (n >= 8 && n < 13) V[n - 5] = cmul5(V[n - 7], V[2], vp[n - 5]);
      if constexpr (n >= 13 && n < 36) {
        constexpr int r = n - 4;
        if constexpr (r % 8 == 0) G = cmul5(G, E[1], vp[r]); else cmul5(V[r % 8], G, vp[r]);
      }
    };
    auto gfill = [&](auto I) { return [&](auto K) { if constexpr (K.v & 1) gop(ic<decltype(I)::v * 5 + K.v / 2>{}); }; };
    __builtin_amdgcn_s_setprio(BPRIO);
    WAITV(10); BAR();
    chunk(ic<0>{}, [&](auto K) {
      gfill(ic<0>{})(K);
      if constexpr (K.v == 2) { __builtin_amdgcn_raw_buffer_store_b128(pub[0], wsr, o_t, 0, SC1); FENCE(); }
      if constexpr (K.v == 8) { __builtin_amdgcn_raw_buffer_store_b128(pub[2], wsr, o_t2, 0, SC1); FENCE(); }
    });
    WAITV(10); BAR();
    chunk(ic<1>{}, [&](auto K) {
      gfill(ic<1>{})(K);
      if constexpr (K.v == 4) { __builtin_amdgcn_raw_buffer_store_b128(pub[1], wsr, o_t + 4096, 0, SC1); FENCE(); }
      if constexpr (K.v == 8) conv_addr_a(cx, lane, wave);
    });
    WAITV(10); BAR();
    chunk(ic<2>{}, gfill(ic<2>{}));
    WAITV(10); BAR();
    flag_store(wsd, wave == 4 ? WS_F1 + (bid >> 2) * 128 + (bid & 3) * 4 : OOB, cx.vtag);
    chunk(ic<3>{}, [&](auto K) { gfill(ic<3>{})(K); if constexpr (K.v == 8) conv_addr_b(cx, tid, wave); });
    WAITV(9); BAR();
    int flo;
    chunk(ic<4>{}, [&](auto K) {
      gfill(ic<4>{})(K);
      if constexpr (K.v == 8) {
        int b0 = bw == 0 ? WS_F1 : bw == 1 ? WS_F3 + gn * 256 : OOB, b1 = WS_F3 + (24 + (gn >> 2)) * 128, lim = bw == 1 ? 2 : 64;
        asm volatile("" : "+s"(b0), "+s"(b1), "+s"(lim));
        flo = lane < lim ? b0 + lane * 128 : b1;
        PIN(flo);
      }
    });
    WAITV(8); BAR();
    v4u fl;
    chunk(ic<5>{}, [&](auto K) { gfill(ic<5>{})(K); if constexpr (K.v == 9) fl = ld_async(wsd, flo, 0); });
    WAITV(11); BAR();
    int o_c;
    chunk(ic<6>{}, [&](auto K) {
      gfill(ic<6>{})(K);
      if constexpr (K.v == 4) {
        ugo = WS_XNB + ch0 * 16 + ((tid & 255) >> 1) * 10240 + (lane & 1) * 16;
        uwo = LDS_U + 512 + (lane & 1) * U_STR + ((tid & 255) >> 1) * 16;
        PIN(ugo); PIN(uwo);
      }
      if constexpr (K.v == 8) { o_c = lane < 16 ? lane16 : OOB; PIN(o_c); }
    });
    asm volatile("s_waitcnt vmcnt(8)" : "+v"(fl) :: "memory");
    if (bw < 2) { if (!all4(fl, tag)) poll4(wsr, flo, tag); }
    BAR();
    v4u ug[2], cf_, rf_[2];
    v3u rc_[2];
    chunk(ic<7>{}, [&](auto K) {
      constexpr int k = K.v;
      if constexpr (k == 0) gop(ic<35>{});
      if constexpr (k == 1) cf_ = ld_async(wsd, o_c, WS_CS + (1536 + gn * 16) * 16);
      if constexpr (k == 2 || k == 3) rf_[k - 2] = ld_async<(k - 2) * 512>(wsd, l15 * 32, WS_STATS + (gm * 128 + bw * 32) * 32);
      if constexpr (k == 4 || k == 5) rc_[k - 4] = ld_async3<16 + (k - 4) * 512>(wsd, l15 * 32, WS_STATS + (gm * 128 + bw * 32) * 32);
      if constexpr (k == 7) {
        zao = lane < 4 ? LDS_KCL + (bw * 2 + (lane >> 1)) * KC_STR + 4096 + (lane & 1) * 16 : LDS_U + (bw & 1) * U_STR + (lane & 31) * 16;
        PIN(zao);
      }
      if constexpr (k == 8) ug[0] = ld_async(wsd, ugo, 0);
      if constexpr (k == 9) ug[1] = ld_async(wsd, ugo, 128 * 10240);
    });
    WAITV(7);
    __builtin_amdgcn_s_setprio(0);
    BAR();
    chunk(ic<8>{}, [&](auto K) {
      constexpr int k = K.v;
      LDV(unsigned, a_gen + k * KROW) = vp[k];
      if constexpr (k % 3 == 0) ldw2r<k / 3, k / 3 + 1>(cx, W2);
    });
    chunk(ic<9>{}, [&](auto K) {
      constexpr int k = K.v;
      LDV(unsigned, a_gen + (10 + 2 * k) * KROW) = vp[10 + 2 * k]; LDV(unsigned, a_gen + (11 + 2 * k) * KROW) = vp[11 + 2 * k];
      if constexpr (k >= 8) LDV(unsigned, a_gen + (22 + k) * KROW) = vp[22 + k];
      if constexpr (k % 3 == 0) ldw2r<4 + k / 3, 5 + k / 3>(cx, W2);
    });
    landed<10>(cf_); PIN(rf_[0]); PIN(rf_[1]); PIN(rc_[0]); PIN(rc_[1]);
    mfma_ip(accs[0], cf_, rf_[0]); mfma_ip(accs[1], cf_, rf_[1]);
    __builtin_amdgcn_s_setprio(1);
    STAMPV(25);
    BAR();
    STAMPV(26);
    {
      v4u fa[2][8], fb[8];
#pragma unroll
      for (int ks = 0; ks < 8; ++ks) { fb[ks] = LD(v4u, kfb + ks * 64); fa[0][ks] = LD(v4u, kfa0 + ks * 64); fa[1][ks] = LD(v4u, kfa1 + ks * 64); }
      LD(unsigned, LDS_KD + hh * KD_STR + (lane & 7) * 4) = 0u;
      SB();
      ldw2r<8, 11>(cx, W2);
      f32x4 d0 = {0.f, 0.f, 0.f, 0.f}, d1 = {0.f, 0.f, 0.f, 0.f};
#pragma unroll
      for (int ks = 0; ks < 8; ++ks) { d0 = mfma(fa[0][ks], fb[ks], d0); d1 = mfma(fa[1][ks], fb[ks], d1); }
      LD(unsigned, kd0) = pk2(d0.x, d0.y); LD(unsigned, kd1) = pk2(d0.z, d0.w);
      unsigned p01 = pk2(d1.x, d1.y), p23 = pk2(d1.z, d1.w);
      LD(u16, kd2) = (u16)p01; LD(u16, kd3 + 64) = (u16)(p01 >> 16); LD(u16, kd3 + 128) = (u16)p23; LD(u16, kd3 + 192) = (u16)(p23 >> 16);
      SB();
      ldw2r<11, 14>(cx, W2);
      BAR();
      STAMPV(27);
      unsigned ev[2][11];
#pragma unroll
      for (int c = 0; c < 2; ++c)
#pragma unroll
        for (int i = 0; i < 11; ++i) ev[c][i] = LD(u16, ead.ex[i] + c * KD_STR);
      landed<14>(ug[0], ug[1]);
      LD(v4u, uwo) = ug[0];
      LD(v4u, uwo + 2048) = ug[1];
      SB();
      {
        const f32x2 ac[4] = {accs[0].xy, accs[0].zw, accs[1].xy, accs[1].zw};
        unsigned hf[4];
        gelu(ac, rc_[0], rc_[1], hf, [&](auto H) { if constexpr ((H.v & 1) == 0) ldw2r<14 + H.v / 2, 15 + H.v / 2>(cx, W2); });
        const int hoffs = WS_HT + ((gm * 8 + bw * 2) * KS2 + 48 + (gn >> 1)) * 1024 + (gn & 1) * 8;
        __builtin_amdgcn_raw_buffer_store_b64((v2u){hf[0], hf[1]}, wsr, lane16, hoffs, SC1);
        __builtin_amdgcn_raw_buffer_store_b64((v2u){hf[2], hf[3]}, wsr, lane16, hoffs + KS2 * 1024, SC1);
        SB();
      }
      kcopies(ev[0], ead.ew);
      SB();
      kcopies(ev[1], ead.ew + 4 * KC_STR);
      LD(v4u, zao) = (v4u){0u, 0u, 0u, 0u};
    }
    __builtin_amdgcn_s_setprio(0);
    STAMPV(28);
    BAR();
    asm volatile("; end of role 1" ::: "memory");
  } else {
    int p0 = (lane >> 3) * 1280 + (lane & 7) * 16;
    PIN(p0);
    const int blk0 = ai * 8 + wave, blk1 = 512 + ai - 36;
    const bool two = wave == 0 && ai >= 36;
    auto stage = [&](int blk, int m0b) {
      const int so = ((blk * 6554) >> 16) * (32 * 1280 - 1280) + blk * 128;
      set_m0(m0b);
      glds_o<0>(w2d, p0, so); glds_o<1024>(w2d, p0, so + 8 * 1280 - 1024); glds_o<2048>(w2d, p0, so + 16 * 1280 - 2048); glds_o<3072>(w2d, p0, so + 24 * 1280 - 3072);
    };
    asm volatile("s_barrier" ::: "memory");
    stage(blk0, m0w);
    STAMPV(1);
    ln_addr();
    misc_addr2();
    kaddr_frag(kad, lane, wave);
    int tx = ((xcd >> 1) * 512 + (slot - 24) * 64 + lane) * 1280;
    int tw = ((slot - 24) * 8 + (lane >> 4)) * 3456 + (l15 < 12 ? (xcd & 1) * 1536 + l15 * 128 : 3072 + ((xcd & 1) + (lane & 1)) * 128);
    PIN(tx); PIN(tw);
    auto touch = [&]() {
      const int kc = wave + PF0;
      touch3(tx, tw, xd, w1d, kc < 10 ? kc * 128 : OOB, kc * (64 * 3456));
    };
    STAMPV(2);
    asm volatile("s_waitcnt vmcnt(4)\n\ts_barrier" ::: "memory");
    STAMPV(3);
    if (two) stage(blk1, 32768);
    if (PFPOS == 0) touch();
    ln_row([&](auto) {});
    cx.vtag = epoch + 1u; PIN(cx.vtag); tag = __builtin_amdgcn_readfirstlane(cx.vtag);
    STAMPV(4);
    if (PFPOS == 0) WAITV(3); else WAITV(0);
    if (PFPOS == 1) touch();
    v4u cw[10];
    const rsrc_t w1r = as_rsrc(w1d);
    const int co = ai < 27 ? (wave * 80 + (lane >> 3)) * 3456 + cg * 128 + (lane & 7) * 16 : OOB;
#pragma unroll
    for (int i = 0; i < 5; ++i) cw[i] = __builtin_amdgcn_raw_buffer_load_b128(w1r, co, i * 8 * 3456, 0);
    FENCE();
    auto tile = [&](int blk, int m0b) {
      const bool strip = blk >= 480;
      const int tp = m0b + ((strip ? 4 * g : (g >> 1) * 16 + (g & 1) * 4) + (l15 >> 2)) * 128 + (lane & 3) * 8, t2 = tp + (strip ? 16 : 8) * 128;
      v2u lo[4], hi[4];
#pragma unroll
      for (int cc = 0; cc < 4; ++cc) { lo[cc] = trread(tp + cc * 32); hi[cc] = trread(t2 + cc * 32); }
      SB();
#pragma unroll
      for (int cc = 0; cc < 4; ++cc)
        __builtin_amdgcn_raw_buffer_store_b128((v4u){lo[cc].x, lo[cc].y, hi[cc].x, hi[cc].y}, wsr, WS_W2T + blk * 4096 + lane16 + cc * 1024, 0, SC1);
      SB();
    };
    wave_sum2(s2, q2);
    ln_stats(s2, q2);
    ln_stats_store(lane == 0 ? WS_STATS + (tb * 8 + wave) * 32 : OOB);
    tile(blk0, m0w);
    if (two) tile(blk1, 32768);
    FENCE();
    BAR();
    ln_publish();
#pragma unroll
    for (int i = 5; i < 10; ++i) cw[i] = __builtin_amdgcn_raw_buffer_load_b128(w1r, co, i * 8 * 3456, 0);
    FENCE();
    STAMPV(5);
    kaddr_scatter(kad, lane, wave);
    eaddr_init(ead, tid, wave);
    eaddr_pair<0>(ead); eaddr_pair<1>(ead); eaddr_pair<2>(ead); eaddr_pair<3>(ead); eaddr_pair<4>(ead); eaddr_pair<5>(ead);
    LD(unsigned, LDS_KD + g * KD_STR + (lane & 7) * 4) = 0u;
    {
      v4u sel;
#pragma unroll
      for (int i = 0; i < 4; ++i) sel[i] = (l15 >> 1) == i ? 0x3f80u << ((lane & 1) * 16) : 0u;
      STAMPV(8);
      SB();
      f32x4 d = {0.f, 0.f, 0.f, 0.f};
#pragma unroll
      for (int i = 0; i < 10; ++i) d = mfma(sel, cw[i], d);
      if (lane < 32) LD(f32x4, LDS_CSP + (wave * 2 + (l15 >> 3)) * 256 + (lane & 7) * 32 + g * 16) = d;
    }
    WAITV(0);
    BAR();
    flag_store(wsd, wave == 0 ? WS_F1 + (bid >> 2) * 128 + (bid & 3) * 4 : OOB, cx.vtag);
    if (wave == 3 && ai < 27) {
      float cs = 0.f;
#pragma unroll
      for (int i = 0; i < 16; ++i) cs += LD(float, LDS_CSP + i * 256 + lane * 4);
      const unsigned hi = pk2(cs, cs), hl = pk2(cs, cs - bflo(hi)), b1v = LD(u16, LDS_B1 + lane * 2);
      __builtin_amdgcn_raw_buffer_store_b128((v4u){hl, (hi & 0xffffu) | (b1v << 16), b1v, 0u}, wsr, WS_CS + cg * 1024 + lane16, 0, SC1);
      WAITV(0);
      if (lane == 0) __builtin_amdgcn_raw_buffer_store_b128((v4u){tag, tag, tag, tag}, wsr, WS_F3 + cg * 128, 0, SC1);
    }
    STAMPV(9);
#pragma unroll 1
    for (int rd = 0; rd < 2; ++rd) {
      float dt = rbf(__builtin_amdgcn_exp2f(bflo(LD(u16, LDS_PAR_AUX + 6 * 2048 + (rd * 2 + kc_) * 2)) * 1.44269504088896f));
      float a2 = dt * bflo(LD(u16, a_par)) * 1.44269504088896f;
      float bt = dt * bflo(LD(u16, a_par + 2048)) * 0.15915494309189535f;
      if (th == 0) {
        f32x2 V[8], G[4];
        int a_gv = a_gen; PIN(a_gv);
        V[0] = (f32x2){1.f, 0.f};
        V[1] = cexp_l(a2, bt, 8.f);
        G[1] = cexp_l(a2, bt, 64.f);
        V[2] = cmul(V[1], V[1]); V[3] = cmul(V[2], V[1]); V[4] = cmul(V[2], V[2]);
        V[5] = cmul(V[4], V[1]); V[6] = cmul(V[4], V[2]); V[7] = cmul(V[5], V[2]);
        G[2] = cmul(G[1], G[1]); G[3] = cmul(G[2], G[1]);
#pragma unroll
        for (int j = 0; j < 8; ++j) LDV(unsigned, a_gv + j * KROW) = pk2(V[j].x, V[j].y);
#pragma unroll
        for (int i = 1; i < 4; ++i) {
          f32x2 t[8];
#pragma unroll
          for (int j = 0; j < 8; ++j) t[j] = j == 0 ? G[i] : cmul(V[j], G[i]);
#pragma unroll
          for (int j = 0; j < 8; ++j) LDV(unsigned, a_gv + (i * 8 + j) * KROW) = pk2(t[j].x, t[j].y);
        }
        FENCE();
      } else {
        f32x2 Bv = {dt * bflo(LD(u16, a_par + 2 * 2048)), -dt * bflo(LD(u16, a_par + 3 * 2048))};
        f32x2 Cv = {bflo(LD(u16, a_par + 4 * 2048)), -bflo(LD(u16, a_par + 5 * 2048))};
        f32x2 W[21];
        W[0] = cmul(Cv, Bv);
        f32x2 E1 = cexp_l(a2, -bt, 1.f), H = cexp_l(a2, -bt, 256.f);
        f32x2 E2 = cmul(E1, E1), E4 = cmul(E2, E2), H2 = cmul(H, H), H4 = cmul(H2, H2);
        W[1] = cmul(W[0], E1); W[2] = cmul(W[0], E2); W[3] = cmul(W[1], E2);
        W[4] = cmul(W[0], E4); W[5] = cmul(W[1], E4); W[6] = cmul(W[2], E4); W[7] = cmul(W[3], E4);
        W[8] = cmul(W[0], H); W[9] = cmul(W[2], H); W[10] = cmul(W[4], H); W[11] = cmul(W[6], H);
        W[12] = cmul(W[0], H2); W[13] = cmul(W[4], H2); W[14] = cmul(W[8], H2); W[15] = cmul(W[10], H2);
        W[16] = cmul(W[0], H4); W[17] = cmul(W[8], H4); W[18] = cmul(W[12], H4); W[19] = cmul(W[14], H4);
        W[20] = cmul(W[16], H4);
#pragma unroll
        for (int j = 0; j < 21; ++j) LDV(unsigned, a_gen + j * KROW) = pk2(W[j].x, W[j].y);
      }
      a_par += 512;
      a_gen += LDS_KS_AUX - LDS_KS;
      if (rd == 0) STAMPV(10);
    }
    STAMPV(12);
    if (wave == 0) poll4(wsr, WS_F1 + lane * 128, tag);
    BAR();
    STAMPV(13);
    misc_addr();
    v4u ug1 = ld_async<32>(wsd, ugo, 0);
    FENCE();
#pragma unroll
    for (int rd = 0; rd < 2; ++rd) {
      v4u fa[8], fb[8];
      const int fa_ = kad.fa + rd * (LDS_KS_AUX - LDS_KS), fb_ = kad.fb + rd * (LDS_KS_AUX - LDS_KS);
#pragma unroll
      for (int ks = 0; ks < 8; ++ks) { fa[ks] = LD(v4u, fa_ + ks * 64); fb[ks] = LD(v4u, fb_ + ks * 64); }
      f32x4 d = {0.f, 0.f, 0.f, 0.f};
#pragma unroll
      for (int ks = 0; ks < 8; ++ks) d = mfma(fa[ks], fb[ks], d);
      if (rd == 1) kscatter<2 * KD_STR>(kad, d); else kscatter<0>(kad, d);
    }
    v4u ug0 = ld_async(wsd, ugo, 0);
    ldw2r<0, 4>(cx, W2);
    BAR();
    STAMPV(14);
#pragma unroll
    for (int rd = 0; rd < 2; ++rd) {
      unsigned ev[11];
#pragma unroll
      for (int i = 0; i < 11; ++i) ev[i] = LD(u16, ead.ex[i] + rd * 2 * KD_STR);
      kcopies(ev, ead.ew + rd * (LDS_KCA - LDS_KCL));
    }
    LD(v4u, zao) = (v4u){0u, 0u, 0u, 0u};
    if (lane < 2) LD(v4u, zao + (LDS_KCA - LDS_KCL)) = (v4u){0u, 0u, 0u, 0u};
    conv_addr_a(cx, lane, wave); conv_addr_b(cx, tid, wave);
    landed<4>(ug0, ug1);
    LD(v4u, uwo + 2 * U_STR) = ug1;
    LD(v4u, uwo) = ug0;
    int tba = cx.tb + (LDS_KCA - LDS_KCL), ub1 = cx.ub + 2 * U_STR;
    PIN(tba); PIN(ub1);
    ldw2r<4, 7>(cx, W2);
    BAR();
    STAMPV(15);
    conv_round<7, W2A, false>(cx, tba, ub1, cx.uf + 2 * U_STR, (ch0 >> 1) + 1, LDS_DK + 4, 0, W2, [](auto) {}, []() {}, []() {} SV);
    STAMPV(28);
    BAR();
    asm volatile("; end of role 2" ::: "memory");
  }
  asm volatile("; common part" ::: "memory");
  STAMPV(33);

  v4u H[7];
  const int hb = WS_HT + (mb * 4 * KS2 + s0) * 1024, hl = wave < 6 ? 6144 : OOB;
  auto ldh = [&](int r, int s) {
    H[s] = __builtin_amdgcn_raw_buffer_load_b128(wsr, lane16 + (s < 6 ? (s & 3) * 1024 : 0), hb + (r ^ hrot) * (KS2 * 1024) + (s < 6 ? (s >> 2) * 4096 : hl), SC1);
  };
  {
    int fho;
    unsigned fh;
    conv_round<W2A, W2B, true>(cx, cx.tb, cx.ub, cx.uf, (ch0 >> 1), LDS_DK, aux ? OOB : WS_FH + (gm * 16 + gn) * 128, W2,
      [&](auto M) {
        if constexpr (M.v == FHM) {
          fho = WS_FH + (mb >> 1) * 2048 + (lane16 < 11 * 16 ? lane16 : 11 * 16) * 8;
          fh = ld_async1(wsd, fho, wave == FHW ? 0 : OOB);
        }
      },
      [&]() {
        ldw2r<W2B, 35>(cx, W2);
        FENCE();
        landed<35 - W2A - (((FHM + 1) / 6) * (W2B - W2A) + 11) / 12>(fh);
        if (wave == FHW) { if (!__all(fh == tag)) poll1(wsr, fho, tag); }
        FENCE();
      },
      [&]() {
        FENCE();
#pragma unroll
        for (int s = 0; s < 7; ++s) ldh(0, s);
        FENCE();
      } SV);
  }
  STAMPV(36);

  {
    int wro[4], wro4, rdo, rdo4, xo, xo4, spo, spo4, slo, swo, fso;
    f32x2 b2a, b24;
    unsigned b2r[2];
    auto g2addr = [&](auto K) {
      constexpr int k = K.v;
      const int l31 = lane & 31, ta = 2 * wave + hh, t4 = (wave & 1) * 8 + (lane >> 3), gp = (lane >> 1) & 3;
      if constexpr (k == 0) { wro[0] = cx.pw + (LDS_P2 - LDS_CP - wave * 4096); wro4 = cx.pw + (LDS_P4 - LDS_CP - wave * 7168); PIN(wro[0]); PIN(wro4); }
      if constexpr (k == 1) { wro[1] = wro[0] ^ 16; wro[2] = wro[0] ^ 32; wro[3] = wro[0] ^ 48; PIN(wro[1]); PIN(wro[2]); PIN(wro[3]); }
      if constexpr (k == 3) { b2r[0] = LD(unsigned, LDS_B2 + l31 * 4); b2r[1] = LD(unsigned, LDS_B2 + 128 + (lane & 7) * 4); }
      if constexpr (k == 6) { rdo = LDS_P2 + (l31 >> 3) * 1024 + gp * 256 + ((ta ^ (4 * gp + (l31 >> 3))) << 4) + (lane & 1) * 8; PIN(rdo); }
      if constexpr (k == 11) { rdo4 = LDS_P4 + ((wave >> 1) ^ hrot) * 8192 + gp * 256 + ((t4 ^ (4 * gp)) << 4) + (lane & 1) * 8; PIN(rdo4); }
      if constexpr (k == 15) { xo = ((mb * 64 + 2 * wave) * 640 + nb * 80) * 2 + hh * 1280 + l31 * 4; PIN(xo); }
      if constexpr (k == 17) { xo4 = ((mb * 64 + 8 * wave) * 640 + nb * 80 + 64) * 2 + (lane >> 3) * 1280 + (lane & 7) * 4; PIN(xo4); }
      if constexpr (k == 20) { spo = LDS_S2 + wave * 8 + l31 * S2_STR + hh * 4; PIN(spo); }
      if constexpr (k == 22) { spo4 = LDS_S2 + 32 * S2_STR + wave * 32 + (lane & 7) * S2_STR + (lane >> 3) * 4; PIN(spo4); }
      if constexpr (k == 25) { slo = WS_ST + ((nb * 40 + wave * 4) * 2048 + mb * 64) * 4 + g * 8192 + l15 * 16; PIN(slo); }
      if constexpr (k == 27) { swo = LDS_S2 + wave * 4 * S2_STR + g * 16 + lane16; PIN(swo); }
      if constexpr (k == 29) { fso = l31 * 1024 + WS_FS + nb * 128; PIN(fso); }
      if constexpr (k == 31) { b2a = unpk(b2r[0]); b24 = unpk(b2r[1]); PIN(b2a); PIN(b24); }
    };
    STAMPV(46);
    const rsrc_t xr = as_rsrc(xd), orr = as_rsrc(od);
    f32x2 zs[3];
    unsigned xv[5];
    v4u sreg[2];
    unsigned fs;
    f32x4 acc[2][5];
    auto wrp = [&](auto R, auto J) {
      constexpr int r = R.v, j = J.v;
      if constexpr (j < 4) LDV(f32x4, wro[j] + (r & 1) * 32768 + j * 1024) = acc[r & 1][j];
      else LDV(f32x4, wro4 + r * 8192) = acc[r & 1][4];
    };
    auto rowtile = [&](auto R) {
      constexpr int r = R.v;
      f32x2 pp[4];
      float e[2];
      static_for<35>([&](auto K) {
        constexpr int k = K.v;
        constexpr int s = k < 25 ? k / 5 : (k == 25 || k == 26 || k == 28 || k == 30 || k == 32) ? 5 : 6;
        constexpr int j = k < 25 ? k % 5 : k == 25 ? 0 : k == 34 ? 4 : s == 5 ? (k - 24) / 2 : (k - 27) / 2;
        if constexpr (s == 0) mfma_z(acc[r & 1][j], W2[s * 5 + j], H[s]); else mfma_ip(acc[r & 1][j], W2[s * 5 + j], H[s]);
        SB();
        if constexpr (r < 3 && k < 25 && k % 5 == 4) ldh(r + 1, k / 5);
        if constexpr (r < 3 && k == 32) ldh(r + 1, 5);
        if constexpr (r < 3 && k == 34) ldh(r + 1, 6);
        if constexpr (r == 0) g2addr(K);
        if constexpr (r > 0) {
          if constexpr (k == 1 || k == 3 || k == 6 || k == 8 || k == 11) wrp(ic<r - 1>{}, ic<(k == 1 ? 0 : k == 3 ? 1 : k == 6 ? 2 : k == 8 ? 3 : 4)>{});
          if constexpr (k == 17) {
            if constexpr (r == 1) WAITV(7);
            if constexpr (r == 3) { if (wave == 7) { if (!__all(fs == tag)) poll1(wsr, fso, tag); } }
            STAMPV(46 + r * 3);
            BAR();
            STAMPV(47 + r * 3);
            if constexpr (r == 1) flag_store(wsd, wave == 0 ? WS_FS + bid * 128 : OOB, cx.vtag);
          }
          constexpr int rk = k < 18 ? -1 : k < 22 ? k - 18 : (k >= 25 && k < 29) ? k - 21 : -1;
          constexpr int ak = (k >= 24 && k < 28) ? k - 24 : (k >= 30 && k < 34) ? k - 26 : -1;
          if constexpr (ak >= 0) zs[r - 1] = vadd2(ak == 0 ? b2a : zs[r - 1], pp[ak & 3]);
          if constexpr (rk >= 0) pp[rk & 3] = LDV(f32x2, rdo + ((r - 1) & 1) * 32768 + rk * 4096);
        }
        if constexpr (r > 1) {
          if constexpr (k == 2 || k == 5) { e[k == 5] = __builtin_amdgcn_exp2f(zs[r - 2][k == 5] * -1.44269504088896f); PIN(e[k == 5]); }
          if constexpr (k == 10 || k == 12) { e[k == 12] = __builtin_amdgcn_rcpf(e[k == 12] + 1.f); PIN(e[k == 12]); }
          if constexpr (k == 15) zs[r - 2] = (f32x2){vmul(zs[r - 2].x, e[0]), vmul(zs[r - 2].y, e[1])};
        }
        if constexpr (r == 3) {
          if constexpr (k == 0) fs = __builtin_amdgcn_raw_buffer_load_b32(wsr, fso, wave == 7 ? 0 : OOB, SC1);
          if constexpr (k == 4 || k == 7 || k == 9 || k == 13) {
            constexpr int i = k == 4 ? 0 : k == 7 ? 1 : k == 9 ? 2 : 3;
            xv[i] = __builtin_amdgcn_raw_buffer_load_b32(xr, xo, (i ^ hrot) * (16 * 1280), 0);
          }
          if constexpr (k == 14) xv[4] = __builtin_amdgcn_raw_buffer_load_b32(xr, xo4, 0, 0);
          if constexpr (k == 17) {
            sreg[0] = __builtin_amdgcn_raw_buffer_load_b128(wsr, slo, 0, SC1);
            sreg[1] = __builtin_amdgcn_raw_buffer_load_b128(wsr, slo, wave < 2 ? 32 * 8192 : OOB, SC1);
          }
        }
        SB();
      });
      STAMPV(48 + r * 3);
    };
    rowtile(ic<0>{}); rowtile(ic<1>{}); rowtile(ic<2>{}); rowtile(ic<3>{});
    wrp(ic<3>{}, ic<0>{});
    asm volatile("s_nop 0" : "+v"(acc[1][1]));
    wrp(ic<3>{}, ic<1>{});
    asm volatile("s_nop 0" : "+v"(acc[1][2]));
    wrp(ic<3>{}, ic<2>{});
    asm volatile("s_nop 1" : "+v"(acc[1][3]));
    wrp(ic<3>{}, ic<3>{});
    asm volatile("s_nop 0" : "+v"(acc[1][4]));
    wrp(ic<3>{}, ic<4>{});
    zs[2] = xsig2(zs[2], zs[2] * -1.44269504088896f);
    f32x2 xf[5];
#pragma unroll
    for (int i = 0; i < 5; ++i) { xf[i] = unpk(xv[i]); PIN(xf[i]); }
    LD(v4u, swo) = sreg[0];
    if (wave < 2) LD(v4u, swo + 32 * S2_STR) = sreg[1];
    BAR();
    STAMPV(58);
    {
      f32x2 pa[8], pc[8];
#pragma unroll
      for (int w = 0; w < 8; ++w) pa[w] = LDV(f32x2, rdo + 32768 + w * 4096);
#pragma unroll
      for (int w = 0; w < 8; ++w) pc[w] = LDV(f32x2, rdo4 + w * 1024);
      unsigned sv[5];
#pragma unroll
      for (int r = 0; r < 4; ++r) sv[r] = LDV(unsigned, (r < 2 ? spo + hrot * 64 : spo - hrot * 64) + r * 64);
      sv[4] = LDV(unsigned, spo4);
      SB();
      auto emit = [&](int i, f32x2 gg) {
        f32x2 o = fma2(unpk(sv[i]), gg, xf[i]);
        __builtin_amdgcn_raw_buffer_store_b32(pk2(o.x, o.y), orr, i < 4 ? xo : xo4, i < 4 ? (i ^ hrot) * (16 * 1280) : 0, 0);
      };
      f32x2 za = (((pa[0] + pa[1]) + (pa[2] + pa[3])) + ((pa[4] + pa[5]) + (pa[6] + pa[7]))) + b2a;
      f32x2 zc = (((pc[0] + pc[1]) + (pc[2] + pc[3])) + ((pc[4] + pc[5]) + (pc[6] + pc[7]))) + b24;
      emit(0, zs[0]); emit(1, zs[1]); emit(2, zs[2]);
      emit(3, xsig2(za, za * -1.44269504088896f));
      emit(4, xsig2(zc, zc * -1.44269504088896f));
    }
  }
}

}

extern "C" void kernel_launch(void* const* d_in, const int* in_sizes, int n_in, void* d_out, int out_size, void* d_ws,
                              size_t ws_size, hipStream_t stream) {
  using namespace fk;
  (void)hipFuncSetAttribute((const void*)fused_kernel, hipFuncAttributeMaxDynamicSharedMemorySize, LDS_TOTAL);
  Args a;
  const int src[13] = {1, 2, 3, 4, 5, 6, 7, 8, 11, 13, 0, 10, 12};
  const unsigned bytes[13] = {D_ * N_ * 2, D_ * N_ * 2, D_ * N_ * 2, D_ * N_ * 2, D_ * N_ * 2, D_ * N_ * 2, D_ * 2, D_ * 2, F_ * 2, D_ * 2,
                              L_ * D_ * 2, D_ * F_ * 2, F_ * D_ * 2};
  for (int i = 0; i < 13; ++i) a.d[i] = Desc{d_in[src[i]], bytes[i], 0x00020000u};
  a.d[13] = Desc{d_out, L_ * D_ * 2, 0x00020000u};
  a.d[14] = Desc{d_ws, WS_END, 0x00020000u};
  fused_kernel<<<256, 512, LDS_TOTAL, stream>>>(a);
}
```
